```python
import torch

def _softmax(x, axis=-1):
    return torch.softmax(x, dim=axis)
def _gelu(x):
    return torch.nn.functional.gelu(x, approximate="tanh")
def _silu(x):
    return torch.nn.functional.silu(x)
def _sigmoid(x):
    return torch.sigmoid(x)
def _layernorm(x, axis=-1, eps=1e-5):
    m = x.mean(dim=axis, keepdim=True)
    v = x.var(dim=axis, keepdim=True, unbiased=False)
    return (x - m) / torch.sqrt(v + eps)
def _scatter_add(tgt, idx, src, axis=0):
    return tgt.clone().index_add_(axis, idx, src)
def _segment_sum(src, seg, n):
    out = torch.zeros((n,) + src.shape[1:], dtype=src.dtype)
    return out.index_add_(0, seg, src)
def _pad(x, pw):
    flat = tuple(v for pair in reversed(pw) for v in pair)
    return torch.nn.functional.pad(x, flat)

kernel_name = 'interleave_layers__compressed_kv_cla__glu_gated_swiglu__decode'

def setup_inputs():

    _w = (lambda a: (lambda t: t.to(torch.bfloat16) if t.is_floating_point() else t)(torch.as_tensor(a)))
    def _A_setup():
        B, H, Sq, Sk, D, M = 2, 32, 1, 8192, 128, 512
        Dc = 4 * D
        return {
            "x": _w((lambda *s: torch.randn(*s, dtype=torch.bfloat16) * 0.05)(B, Sq, M)),
            "c_kv": _w((lambda *s: torch.randn(*s, dtype=torch.bfloat16) * 0.8)(B, Sk, Dc)),
            "w_q": _w((lambda *s: torch.randn(*s, dtype=torch.bfloat16) * 1.6)(M, H, D) * (M ** -0.5)),
            "w_uk": _w((lambda *s: torch.randn(*s, dtype=torch.bfloat16) * 1.6)(Dc, H, D) * (Dc ** -0.5)),
            "w_uv": _w((lambda *s: torch.randn(*s, dtype=torch.bfloat16) * 0.8)(Dc, H, D) * (Dc ** -0.5)),
            "w_o": _w((lambda *s: torch.randn(*s, dtype=torch.bfloat16) * 0.8)(H, D, M) * ((H * D) ** -0.5)),
        }
    _A_in = _A_setup()

    def _B_setup():
        B, S, D, F = 2, 1, 512, 1792
        return {
            "x": _w((lambda *s: torch.randn(*s, dtype=torch.bfloat16) * 0.05)(B, S, D)),
            "w1": _w((lambda *s: torch.randn(*s, dtype=torch.bfloat16) * 0.4)(D, F) * (2.0 / D) ** 0.5),
            "w3": _w((lambda *s: torch.randn(*s, dtype=torch.bfloat16) * 0.4)(D, F) * (2.0 / D) ** 0.5),
            "w2": _w((lambda *s: torch.randn(*s, dtype=torch.bfloat16) * 0.4)(F, D) * (2.0 / F) ** 0.5),
        }
    _B_in = _B_setup()

    return {
        "A_x": _A_in["x"],
        "A_c_kv": _A_in["c_kv"],
        "A_w_q": _A_in["w_q"],
        "A_w_uk": _A_in["w_uk"],
        "A_w_uv": _A_in["w_uv"],
        "A_w_o": _A_in["w_o"],
        "B_x": _B_in["x"],
        "B_w1": _B_in["w1"],
        "B_w3": _B_in["w3"],
        "B_w2": _B_in["w2"],
    }

def reference(A_x, A_c_kv, A_w_q, A_w_uk, A_w_uv, A_w_o, B_x, B_w1, B_w3, B_w2):
    def _A_ref(x, c_kv, w_q, w_uk, w_uv, w_o):
        B, Sq, M = x.shape[0], x.shape[1], x.shape[2]
        Sk = c_kv.shape[1]
        H, D = w_q.shape[1], w_q.shape[2]
        q = torch.einsum("bqm,mhd->bhqd", x, w_q)
        k = torch.einsum("bkc,chd->bhkd", c_kv, w_uk)
        v = torch.einsum("bkc,chd->bhkd", c_kv, w_uv)
        scores = torch.einsum("bhqd,bhkd->bhqk", q, k) * (D ** -0.5)
        off = Sk - Sq
        qi = torch.arange(Sq)[:, None]
        kj = torch.arange(Sk)[None, :]
        cmask = kj <= (qi + off)
        scores = torch.where(cmask[None, None], scores, -1e9)
        p = _softmax(scores, axis=-1)
        ctx = torch.einsum("bhqk,bhkd->bhqd", p, v)
        return torch.einsum("bhqd,hdm->bqm", ctx, w_o)

    def _B_ref(x, w1, w3, w2):
        gate = torch.einsum("bsd,df->bsf", x, w1)
        up = torch.einsum("bsd,df->bsf", x, w3)
        h = _silu(gate) * up
        out = torch.einsum("bsf,fd->bsd", h, w2)
        return out

    x = A_x
    x = x + _A_ref(_layernorm(x, axis=-1), A_c_kv, A_w_q, A_w_uk, A_w_uv, A_w_o)
    x = x + _B_ref(_layernorm(x, axis=-1), B_w1, B_w3, B_w2)
    x = x + _A_ref(_layernorm(x, axis=-1), A_c_kv, A_w_q, A_w_uk, A_w_uv, A_w_o)
    x = x + _B_ref(_layernorm(x, axis=-1), B_w1, B_w3, B_w2)
    return x
```

```cpp
#include <hip/hip_runtime.h>
#include <hip/hip_bf16.h>

typedef unsigned short u16;
__device__ __forceinline__ float bf2f(u16 b) { return __uint_as_float(((unsigned)b) << 16); }
__device__ __forceinline__ u16 f2bf(float f) { unsigned u = __float_as_uint(f); u += 0x7fffu + ((u >> 16) & 1u); return (u16)(u >> 16); }
__device__ __forceinline__ float rbf(float f) { return bf2f(f2bf(f)); }

constexpr int B = 2, H = 32, SK = 8192, D = 128, M = 512, DC = 512, F = 1792;

__device__ void layernorm_block(const float* xg, float* xn  , float* red  ) {
    const int tid = threadIdx.x;
    for (int b = 0; b < B; ++b) {
        float a0 = xg[b * M + tid], a1 = xg[b * M + tid + 256];
        float s = a0 + a1;
        for (int o = 32; o > 0; o >>= 1) s += __shfl_xor(s, o);
        if ((tid & 63) == 0) red[tid >> 6] = s;
        __syncthreads();
        float mean = (red[0] + red[1] + red[2] + red[3]) * (1.0f / M);
        __syncthreads();
        float d0 = a0 - mean, d1 = a1 - mean;
        float v = d0 * d0 + d1 * d1;
        for (int o = 32; o > 0; o >>= 1) v += __shfl_xor(v, o);
        if ((tid & 63) == 0) red[tid >> 6] = v;
        __syncthreads();
        float var = (red[0] + red[1] + red[2] + red[3]) * (1.0f / M);
        __syncthreads();
        float mb = rbf(mean), vb = rbf(var);
        float sd = rbf(sqrtf(rbf(vb + 1e-5f)));
        xn[b * M + tid] = rbf(rbf(a0 - mb) / sd);
        xn[b * M + tid + 256] = rbf(rbf(a1 - mb) / sd);
    }
    __syncthreads();
}

__global__ void k_init(const u16* x, float* xf) { int i = blockIdx.x * 256 + threadIdx.x; if (i < B * M) xf[i] = bf2f(x[i]); }

__global__ __launch_bounds__(256) void k_qabs(const float* xf, const u16* wq, const u16* wuk, float* qabs) {
    __shared__ float xn[B * M]; __shared__ float red[4]; __shared__ float q[B][D]; __shared__ float part[2][B][D];
    layernorm_block(xf, xn, red);
    const int h = blockIdx.x, cs = blockIdx.y, tid = threadIdx.x;
    const int d = tid & 127, half = tid >> 7;
    float a0 = 0.f, a1 = 0.f;
    for (int m = half * 256; m < half * 256 + 256; ++m) {
        float w = bf2f(wq[(size_t)m * (H * D) + h * D + d]);
        a0 += xn[m] * w; a1 += xn[M + m] * w;
    }
    part[half][0][d] = a0; part[half][1][d] = a1;
    __syncthreads();
    if (tid < 128) { q[0][d] = rbf(part[0][0][d] + part[1][0][d]); q[1][d] = rbf(part[0][1][d] + part[1][1][d]); }
    __syncthreads();
    const int c = cs * 64 + (tid >> 2), qd = tid & 3;
    float s0 = 0.f, s1 = 0.f;
    for (int dd = qd * 32; dd < qd * 32 + 32; ++dd) {
        float w = bf2f(wuk[(size_t)c * (H * D) + h * D + dd]);
        s0 += q[0][dd] * w; s1 += q[1][dd] * w;
    }
    s0 += __shfl_xor(s0, 1); s0 += __shfl_xor(s0, 2); s1 += __shfl_xor(s1, 1); s1 += __shfl_xor(s1, 2);
    if (qd == 0) { qabs[(0 * H + h) * DC + c] = s0; qabs[(1 * H + h) * DC + c] = s1; }
}

__global__ __launch_bounds__(256) void k_attn(const float* qabs, const u16* ckv, float* opart, float* mpart, float* lpart) {
    __shared__ u16 cs[64][DC + 8];
    __shared__ float qs[H][DC + 4];
    __shared__ float sc[H][64];
    const int b = blockIdx.x >> 7, sp = blockIdx.x & 127, tid = threadIdx.x;
    for (int i = tid; i < 64 * DC / 8; i += 256) {
        int r = i / (DC / 8), cc = i % (DC / 8);
        *(uint4*)&cs[r][cc * 8] = *(const uint4*)&ckv[((size_t)b * SK + sp * 64 + r) * DC + cc * 8];
    }
    for (int i = tid; i < H * DC; i += 256) qs[i / DC][i % DC] = qabs[(size_t)b * H * DC + i];
    __syncthreads();
    const float scale = 0.08838834764831845f;
    for (int i = tid; i < H * 64; i += 256) {
        int h = i >> 6, k = i & 63;
        float s = 0.f;
        for (int c = 0; c < DC; ++c) s += qs[h][c] * bf2f(cs[k][c]);
        sc[h][k] = s * scale;
    }
    __syncthreads();
    if (tid < H) {
        float mx = -1e30f; for (int k = 0; k < 64; ++k) mx = fmaxf(mx, sc[tid][k]);
        float l = 0.f; for (int k = 0; k < 64; ++k) { float p = __expf(sc[tid][k] - mx); sc[tid][k] = p; l += p; }
        mpart[(b * 128 + sp) * H + tid] = mx; lpart[(b * 128 + sp) * H + tid] = l;
    }
    __syncthreads();
    for (int i = tid; i < H * DC; i += 256) {
        int h = i / DC, c = i % DC;
        float o = 0.f;
        for (int k = 0; k < 64; ++k) o += sc[h][k] * bf2f(cs[k][c]);
        opart[((size_t)(b * 128 + sp) * H + h) * DC + c] = o;
    }
}

__global__ __launch_bounds__(256) void k_ctx(const float* opart, const float* mpart, const float* lpart, const u16* wuv, float* ctx) {
    __shared__ float w[128]; __shared__ float ol[DC]; __shared__ float part[2][D]; __shared__ float linv;
    const int h = blockIdx.x, b = blockIdx.y, tid = threadIdx.x;
    if (tid < 64) {
        float m0 = mpart[(b * 128 + tid) * H + h], m1 = mpart[(b * 128 + tid + 64) * H + h];
        float mx = fmaxf(m0, m1);
        for (int o = 32; o > 0; o >>= 1) mx = fmaxf(mx, __shfl_xor(mx, o));
        float w0 = __expf(m0 - mx), w1 = __expf(m1 - mx);
        float l = w0 * lpart[(b * 128 + tid) * H + h] + w1 * lpart[(b * 128 + tid + 64) * H + h];
        for (int o = 32; o > 0; o >>= 1) l += __shfl_xor(l, o);
        w[tid] = w0; w[tid + 64] = w1;
        if (tid == 0) linv = 1.0f / l;
    }
    __syncthreads();
    for (int c = tid; c < DC; c += 256) {
        float o = 0.f;
        for (int s = 0; s < 128; ++s) o += w[s] * opart[((size_t)(b * 128 + s) * H + h) * DC + c];
        ol[c] = o * linv;
    }
    __syncthreads();
    const int d = tid & 127, half = tid >> 7;
    float a = 0.f;
    for (int c = half * 256; c < half * 256 + 256; ++c) a += ol[c] * bf2f(wuv[(size_t)c * (H * D) + h * D + d]);
    part[half][d] = a;
    __syncthreads();
    if (tid < 128) ctx[(b * H + h) * D + d] = rbf(part[0][d] + part[1][d]);
}

__global__ __launch_bounds__(256) void k_wo(const float* ctx, const u16* wo, float* xf) {
    __shared__ float red[2][32][8];
    const int tid = threadIdx.x, col = blockIdx.x * 8 + (tid & 7), kg = tid >> 3;
    float a0 = 0.f, a1 = 0.f;
    for (int k = kg; k < H * D; k += 32) {
        float w = bf2f(wo[(size_t)k * M + col]);
        a0 += ctx[k] * w; a1 += ctx[H * D + k] * w;
    }
    red[0][kg][tid & 7] = a0; red[1][kg][tid & 7] = a1;
    __syncthreads();
    if (tid < 16) {
        int b = tid >> 3, c = tid & 7; float s = 0.f;
        for (int i = 0; i < 32; ++i) s += red[b][i][c];
        int idx = b * M + blockIdx.x * 8 + c;
        xf[idx] = rbf(xf[idx] + rbf(s));
    }
}

__global__ __launch_bounds__(256) void k_gu(const float* xf, const u16* w1, const u16* w3, float* hbuf) {
    __shared__ float xn[B * M]; __shared__ float red[4]; __shared__ float r2[4][32][8];
    layernorm_block(xf, xn, red);
    const int tid = threadIdx.x, col = blockIdx.x * 8 + (tid & 7), kg = tid >> 3;
    float g0 = 0.f, g1 = 0.f, u0 = 0.f, u1 = 0.f;
    for (int k = kg; k < M; k += 32) {
        float a = bf2f(w1[(size_t)k * F + col]), c = bf2f(w3[(size_t)k * F + col]);
        g0 += xn[k] * a; g1 += xn[M + k] * a; u0 += xn[k] * c; u1 += xn[M + k] * c;
    }
    r2[0][kg][tid & 7] = g0; r2[1][kg][tid & 7] = g1; r2[2][kg][tid & 7] = u0; r2[3][kg][tid & 7] = u1;
    __syncthreads();
    if (tid < 16) {
        int b = tid >> 3, c = tid & 7; float g = 0.f, u = 0.f;
        for (int i = 0; i < 32; ++i) { g += r2[b][i][c]; u += r2[2 + b][i][c]; }
        g = rbf(g); u = rbf(u);
        float s = rbf(g / (1.0f + __expf(-g)));
        hbuf[b * F + blockIdx.x * 8 + c] = rbf(s * u);
    }
}

__global__ __launch_bounds__(256) void k_down(const float* hbuf, const u16* w2, float* xf, u16* out, int last) {
    __shared__ float red[2][32][8];
    const int tid = threadIdx.x, col = blockIdx.x * 8 + (tid & 7), kg = tid >> 3;
    float a0 = 0.f, a1 = 0.f;
    for (int k = kg; k < F; k += 32) {
        float w = bf2f(w2[(size_t)k * M + col]);
        a0 += hbuf[k] * w; a1 += hbuf[F + k] * w;
    }
    red[0][kg][tid & 7] = a0; red[1][kg][tid & 7] = a1;
    __syncthreads();
    if (tid < 16) {
        int b = tid >> 3, c = tid & 7; float s = 0.f;
        for (int i = 0; i < 32; ++i) s += red[b][i][c];
        int idx = b * M + blockIdx.x * 8 + c;
        float v = rbf(xf[idx] + rbf(s));
        xf[idx] = v;
        if (last) out[idx] = f2bf(v);
    }
}

extern "C" void kernel_launch(void* const* d_in, const int* in_sizes, int n_in,
                              void* d_out, int out_size, void* d_ws, size_t ws_size,
                              hipStream_t stream) {
    const u16* x = (const u16*)d_in[0]; const u16* ckv = (const u16*)d_in[1];
    const u16* wq = (const u16*)d_in[2]; const u16* wuk = (const u16*)d_in[3];
    const u16* wuv = (const u16*)d_in[4]; const u16* wo = (const u16*)d_in[5];
    const u16* w1 = (const u16*)d_in[7]; const u16* w3 = (const u16*)d_in[8]; const u16* w2 = (const u16*)d_in[9];
    float* ws = (float*)d_ws;
    float* xf = ws; float* qabs = xf + 1024; float* mpart = qabs + B * H * DC; float* lpart = mpart + 256 * H;
    float* ctx = lpart + 256 * H; float* hbuf = ctx + B * H * D; float* opart = hbuf + 4096;
    k_init<<<4, 256, 0, stream>>>(x, xf);
    for (int l = 0; l < 2; ++l) {
        k_qabs<<<dim3(32, 8), 256, 0, stream>>>(xf, wq, wuk, qabs);
        k_attn<<<256, 256, 0, stream>>>(qabs, ckv, opart, mpart, lpart);
        k_ctx<<<dim3(32, 2), 256, 0, stream>>>(opart, mpart, lpart, wuv, ctx);
        k_wo<<<64, 256, 0, stream>>>(ctx, wo, xf);
        k_gu<<<224, 256, 0, stream>>>(xf, w1, w3, hbuf);
        k_down<<<64, 256, 0, stream>>>(hbuf, w2, xf, (u16*)d_out, l == 1);
    }
}
```

```cpp
#include <hip/hip_runtime.h>
#include <hip/hip_bf16.h>
#include <type_traits>

typedef unsigned short u16;
typedef unsigned v4u __attribute__((ext_vector_type(4)));
typedef unsigned v2u __attribute__((ext_vector_type(2)));
typedef float f32x4 __attribute__((ext_vector_type(4)));
typedef short bf16x8 __attribute__((ext_vector_type(8)));
typedef __bf16 bf16x2 __attribute__((ext_vector_type(2)));
#define GAS __attribute__((address_space(1)))
#define LAS __attribute__((address_space(3)))
#define DI __device__ __forceinline__
#define RLX_AGENT __ATOMIC_RELAXED, __HIP_MEMORY_SCOPE_AGENT

#define STAMPS 0

namespace mk {
constexpr int NWG = 256, NT = 512;
constexpr int SK = 8192, DC = 512, MD = 512, HD = 4096, FF = 1792;
constexpr int WS_CTRL = 0;
constexpr int WS_E1 = 256;
constexpr int WS_ST = WS_E1 + 65536;
constexpr int WS_E4 = WS_ST + 16384;
constexpr int WS_E5 = WS_E4 + 4096;
constexpr int WS_E6 = WS_E5 + 14336;
constexpr int WS_ZERO_BYTES = WS_E6 + 4096;
constexpr int WS_E2 = 131072;
constexpr int WS_E3 = WS_E2 + 256 * 16384;
constexpr int WS_STAMP = WS_E3 + 32 * 32768;
constexpr int WS_BYTES = WS_STAMP + 256 * 64 * 8;
static_assert(WS_ZERO_BYTES <= WS_E2 && (WS_ZERO_BYTES % 16) == 0, "ws map");
constexpr int L_QA = 131072;
constexpr int L_QA_BYTES = 17408;
constexpr int L_P = L_QA + L_QA_BYTES;
constexpr int L_SMAX = L_P + 4352;
constexpr int L_SSUM = L_SMAX + 512;
constexpr int L_RED = L_SSUM + 512;
constexpr int L_XS = L_RED + 4096;
constexpr int L_LN = L_XS + 4096;
constexpr int L_X2 = L_LN + 128;
constexpr int L_BYTES = L_X2 + 512;
static_assert(L_BYTES <= 163840, "LDS");

struct Params { const u16 *x, *ckv, *wq, *wuk, *wuv, *wo, *w1, *w3, *w2; u16* out; unsigned char* ws; };

DI float u2f(unsigned u) { return __builtin_bit_cast(float, u); }
DI unsigned f2u(float f) { return __builtin_bit_cast(unsigned, f); }
DI f32x4 mfma(v4u a, v4u b, f32x4 c) {
    return __builtin_amdgcn_mfma_f32_16x16x32_bf16(__builtin_bit_cast(bf16x8, a), __builtin_bit_cast(bf16x8, b), c, 0, 0, 0);
}
DI unsigned pack2(float a, float b) { bf16x2 v = {(__bf16)a, (__bf16)b}; return __builtin_bit_cast(unsigned, v); }
DI float bflo(unsigned u) { return u2f(u << 16); }
DI float bfhi(unsigned u) { return u2f(u & 0xffff0000u); }
DI unsigned pack2_tag(float a, float b, unsigned T) {
    const unsigned u = f2u(a);
    const unsigned lo = T ? (((u >> 17) << 1) | 1u) : (((u + 0x10000u) >> 17) << 1);
    return (lo & 0xffffu) | (pack2(0.f, b) & 0xffff0000u);
}
DI unsigned tagf(float a, unsigned T) { return (f2u(a) & ~1u) | T; }
DI v4u ldg(const u16* p) { return *(const GAS v4u*)p; }
DI v4u ldnt(const u16* p) { return __builtin_nontemporal_load((const GAS v4u*)p); }
DI void glds16(const void* gsrc, unsigned lds_dst) {
    unsigned keep;
    asm volatile("s_mov_b32 %0, m0\n\ts_mov_b32 m0, %2\n\ts_nop 0\n\tglobal_load_lds_dwordx4 %1, off nt\n\ts_mov_b32 m0, %0" : "=&s"(keep) : "v"(gsrc), "s"(lds_dst) : "memory");
}
#define LDNT_ASM(dst, ptr, OFF) asm volatile("global_load_dwordx4 %0, %1, off offset:" #OFF " nt" : "=v"(dst) : "v"(ptr) : "memory")
DI int swz16(int r) { return ((r >> 3) & 1) | ((r & 3) << 1) | ((((r >> 2) ^ (r >> 3)) & 1) << 3); }
DI v4u tr2(unsigned a0, unsigned a1) {
    v2u x, y;
    asm volatile("ds_read_b64_tr_b16 %0, %2\n\tds_read_b64_tr_b16 %1, %3\n\ts_waitcnt lgkmcnt(0)" : "=&v"(x), "=&v"(y) : "v"(a0), "v"(a1) : "memory");
    return v4u{x.x, x.y, y.x, y.y};
}
DI void tr8(v4u (&f)[4], const unsigned (&a)[8]) {
    v2u r0, r1, r2, r3, r4, r5, r6, r7;
    asm volatile("ds_read_b64_tr_b16 %0, %8\n\tds_read_b64_tr_b16 %1, %9\n\tds_read_b64_tr_b16 %2, %10\n\tds_read_b64_tr_b16 %3, %11\n\t"
                 "ds_read_b64_tr_b16 %4, %12\n\tds_read_b64_tr_b16 %5, %13\n\tds_read_b64_tr_b16 %6, %14\n\tds_read_b64_tr_b16 %7, %15\n\ts_waitcnt lgkmcnt(0)"
                 : "=&v"(r0), "=&v"(r1), "=&v"(r2), "=&v"(r3), "=&v"(r4), "=&v"(r5), "=&v"(r6), "=&v"(r7)
                 : "v"(a[0]), "v"(a[1]), "v"(a[2]), "v"(a[3]), "v"(a[4]), "v"(a[5]), "v"(a[6]), "v"(a[7]) : "memory");
    f[0] = v4u{r0.x, r0.y, r1.x, r1.y}; f[1] = v4u{r2.x, r2.y, r3.x, r3.y}; f[2] = v4u{r4.x, r4.y, r5.x, r5.y}; f[3] = v4u{r6.x, r6.y, r7.x, r7.y};
}

template <int CTRL> DI float dpp(float v) { return u2f((unsigned)__builtin_amdgcn_update_dpp(0, (int)f2u(v), CTRL, 0xf, 0xf, true)); }
DI float row_sum16(float v) { v += dpp<0xB1>(v); v += dpp<0x4E>(v); v += dpp<0x141>(v); v += dpp<0x140>(v); return v; }
DI float row_max16(float v) { v = fmaxf(v, dpp<0xB1>(v)); v = fmaxf(v, dpp<0x4E>(v)); v = fmaxf(v, dpp<0x141>(v)); v = fmaxf(v, dpp<0x140>(v)); return v; }
DI float x16_sum(float v) { auto r = __builtin_amdgcn_permlane16_swap(f2u(v), f2u(v), false, false); return u2f(r[0]) + u2f(r[1]); }
DI float x32_sum(float v) { auto r = __builtin_amdgcn_permlane32_swap(f2u(v), f2u(v), false, false); return u2f(r[0]) + u2f(r[1]); }
DI float x16_max(float v) { auto r = __builtin_amdgcn_permlane16_swap(f2u(v), f2u(v), false, false); return fmaxf(u2f(r[0]), u2f(r[1])); }
DI float x32_max(float v) { auto r = __builtin_amdgcn_permlane32_swap(f2u(v), f2u(v), false, false); return fmaxf(u2f(r[0]), u2f(r[1])); }
DI float wave_sum(float v) { return x32_sum(x16_sum(row_sum16(v))); }
DI float wave_max(float v) { return x32_max(x16_max(row_max16(v))); }
DI void pin(v4u& v) { asm volatile("" : "+v"(v)); }
DI void nap(int n) { for (int i = 0; i < n; ++i) __builtin_amdgcn_s_sleep(1); }

struct Ctx {
    __amdgpu_buffer_rsrc_t rs;
    GAS unsigned* abortw;
    unsigned long long t0;
    bool dead;
};
DI v4u ld16(const Ctx& c, int off) { return __builtin_amdgcn_raw_buffer_load_b128(c.rs, off, 0, 16); }
DI v2u ld8(const Ctx& c, int off) { return __builtin_amdgcn_raw_buffer_load_b64(c.rs, off, 0, 16); }
DI unsigned ld4(const Ctx& c, int off) { return __builtin_amdgcn_raw_buffer_load_b32(c.rs, off, 0, 16); }
DI void st16(const Ctx& c, int off, v4u v) { __builtin_amdgcn_raw_buffer_store_b128(v, c.rs, off, 0, 16); }
DI void st8(const Ctx& c, int off, v2u v) { __builtin_amdgcn_raw_buffer_store_b64(v, c.rs, off, 0, 16); }
DI void st4(const Ctx& c, int off, unsigned v) { __builtin_amdgcn_raw_buffer_store_b32(v, c.rs, off, 0, 16); }
DI bool give_up(Ctx& c, int& spins) {
    if (c.dead) return true;
    if ((++spins & 63) == 0) {
        const bool late = (long long)(__builtin_amdgcn_s_memrealtime() - c.t0) > 5000000ll;
        if (late || __hip_atomic_load(c.abortw, RLX_AGENT) != 0u) { __hip_atomic_store(c.abortw, 1u, RLX_AGENT); c.dead = true; return true; }
    }
    return false;
}
DI bool ok4(v4u v, unsigned T) { return (((v.x ^ T) | (v.y ^ T) | (v.z ^ T) | (v.w ^ T)) & 1u) == 0u; }
DI bool ok2(v2u v, unsigned T) { return (((v.x ^ T) | (v.y ^ T)) & 1u) == 0u; }

DI void ln_frags(float x0, float x1, LAS unsigned char* L, int w, int lane, v4u (&fr)[2]) {
    const int n = lane & 15, g = lane >> 4;
    const float m0 = wave_sum(x0) * (1.f / 64), m1 = wave_sum(x1) * (1.f / 64);
    const float d0 = x0 - m0, d1 = x1 - m1;
    const float q0 = wave_sum(d0 * d0), q1 = wave_sum(d1 * d1);
    LAS f32x4* st = (LAS f32x4*)(L + L_LN);
    if (lane == 0) st[w] = f32x4{m0, m1, q0, q1};
    __syncthreads();
    f32x4 s[8];
#pragma unroll
    for (int i = 0; i < 8; ++i) s[i] = st[i];
    float a0 = 0.f, a1 = 0.f;
#pragma unroll
    for (int i = 0; i < 8; ++i) { a0 += s[i].x; a1 += s[i].y; }
    a0 *= 0.125f; a1 *= 0.125f;
    float v0 = 0.f, v1 = 0.f;
#pragma unroll
    for (int i = 0; i < 8; ++i) { const float e0 = s[i].x - a0, e1 = s[i].y - a1; v0 += s[i].z + 64.f * e0 * e0; v1 += s[i].w + 64.f * e1 * e1; }
    const float r0 = 1.0f / sqrtf(v0 * (1.f / 512) + 1e-5f), r1 = 1.0f / sqrtf(v1 * (1.f / 512) + 1e-5f);
    const float y0 = (x0 - a0) * r0, y1 = (x1 - a1) * r1;
    const unsigned hi = pack2(y0, y1);
    const unsigned lo = pack2(y0 - bflo(hi), y1 - bfhi(hi));
    LAS u16* xs = (LAS u16*)(L + L_XS + w * 512);
    const int pos = 32 * (lane >> 5) + 8 * ((lane >> 2) & 3) + 4 * ((lane >> 4) & 1) + (lane & 3);
    xs[pos] = (u16)hi; xs[64 + pos] = (u16)(hi >> 16); xs[128 + pos] = (u16)lo; xs[192 + pos] = (u16)(lo >> 16);
    __builtin_amdgcn_wave_barrier();
#pragma unroll
    for (int ks = 0; ks < 2; ++ks) {
        v4u v = *(LAS v4u*)(L + L_XS + w * 512 + (n & 3) * 128 + 64 * ks + 16 * g);
        if (n >= 4) v = v4u{0u, 0u, 0u, 0u};
        fr[ks] = v;
    }
}

#define IDS() int tid = threadIdx.x; asm volatile("" : "+v"(tid)); const int lane = tid & 63, n = lane & 15, g = lane >> 4; (void)n; (void)g; (void)lane
#define FENCE() __builtin_amdgcn_sched_barrier(0)
#define STAMP(i) do {} while (0)

#define D_E1A 24
#define D_E1 14
#define D_E2 28
#define D_E3 20
#define D_E4 50
#define D_E5 50
#define D_E6 130
#define D_E6R 14
#define D_E6G 50

__global__ void __launch_bounds__(NT, 2) mega(Params p) {
    __shared__ __attribute__((aligned(16))) unsigned char smem[L_BYTES];
    LAS unsigned char* L = (LAS unsigned char*)smem;
    const int tid = threadIdx.x, lane = tid & 63, w = __builtin_amdgcn_readfirstlane(tid >> 6), n = lane & 15, g = lane >> 4;
    const int cu = blockIdx.x, xcd = cu & 7, idx = cu >> 3;
    const int h1 = xcd * 4 + (idx >> 3), j1 = idx & 7;
    const int gs = xcd * 16 + ((idx & 7) | ((idx >> 4) << 3)), bb = gs >> 6, split = gs & 63, hg = 1 - ((idx >> 3) & 1);
    const bool isRed = idx < 4, isGU = idx >= 8 && idx < 15;
    const int red = xcd * 4 + idx, gu = xcd * 7 + (idx - 8);
    unsigned cwarm = 0;
    {
        unsigned long long pc; unsigned len;
        asm volatile("mk_code_begin:\n\ts_getpc_b64 %0\n\ts_mov_b32 %1, mk_code_end-mk_code_begin" : "=s"(pc), "=s"(len));
        const unsigned long long ca = pc + (((unsigned)tid * 128u < len) ? (unsigned)tid * 128u : 0u);
        asm volatile("global_load_dword %0, %1, off" : "=v"(cwarm) : "v"(ca) : "memory");
    }
    Ctx c;
    c.rs = __builtin_amdgcn_make_buffer_rsrc((void*)p.ws, 0, WS_BYTES, 0x00020000);
    c.abortw = (GAS unsigned*)(p.ws + WS_CTRL);
    c.t0 = __builtin_amdgcn_s_memrealtime();
    c.dead = false;
    STAMP(0);

    v4u Ilo, Ihi;
    {
        unsigned a[4], b[4];
#pragma unroll
        for (int q = 0; q < 4; ++q) {
            const int k0 = 8 * g + 2 * q;
            a[q] = (k0 == n ? 0x3f80u : 0u) | (k0 + 1 == n ? 0x3f800000u : 0u);
            b[q] = (k0 == n + 16 ? 0x3f80u : 0u) | (k0 + 1 == n + 16 ? 0x3f800000u : 0u);
        }
        Ilo = v4u{a[0], a[1], a[2], a[3]}; Ihi = v4u{b[0], b[1], b[2], b[3]};
    }
    const f32x4 Z4 = {0.f, 0.f, 0.f, 0.f};

    v4u ck[16];
    v4u ckT[4][4];
    v4u glf[8];
    const u16* ckv0 = p.ckv + ((size_t)bb * SK + (size_t)split * 128) * DC;
    unsigned xr0, xr1, xr2;
    {
        const u16* px = p.x + tid;
        const u16* pr = p.x + ((tid & 1) * MD + 16 * (red & 31) + ((tid >> 1) & 15));
        asm volatile("global_load_ushort %0, %3, off\n\tglobal_load_ushort %1, %3, off offset:1024\n\tglobal_load_ushort %2, %4, off"
                     : "=&v"(xr0), "=&v"(xr1), "=&v"(xr2) : "v"(px), "v"(pr) : "memory");
    }

    v4u wqk[2][4];
    const unsigned lds0 = (unsigned)(size_t)smem;
    {
        v4u aq[4][4];
        if (w >= 4) {
#pragma unroll
            for (int i = 0; i < 4; ++i) {
                const int r = 16 * (w - 4) + 4 * i + g;
                glds16(p.wuk + (size_t)(64 * j1 + r) * HD + h1 * 128 + ((n ^ swz16(r & 15)) * 8), lds0 + L_QA + (16 * (w - 4) + 4 * i) * 256);
            }
        }
        {
            const u16* pq = p.wq + (size_t)(64 * w + n) * HD + h1 * 128 + 8 * g;
#pragma unroll
            for (int mt = 0; mt < 4; ++mt) {
                const u16* pp = pq + (size_t)mt * 16 * HD;
                LDNT_ASM(aq[mt][0], pp, 0); LDNT_ASM(aq[mt][1], pp, 64); LDNT_ASM(aq[mt][2], pp, 128); LDNT_ASM(aq[mt][3], pp, 192);
            }
        }
        if (w < 4) {
#pragma unroll 4
            for (int i = 0; i < 32; ++i) {
                const int r = 32 * w + i;
                glds16(ckv0 + (size_t)r * DC + ((lane ^ swz16(i & 15)) * 8), lds0 + r * 1024);
            }
        } else {
            asm volatile("s_waitcnt vmcnt(16)" ::: "memory");
            const v4u z = {0u, 0u, 0u, 0u};
            const int t = tid - 256;
            const int ib = WS_E2 + (h1 * 8 + j1) * 16384 + t * 16;
#pragma unroll
            for (int i = 0; i < 4; ++i) st16(c, ib + i * 4096, z);
            if (isRed) {
                const int rb = WS_E3 + red * 32768 + t * 16;
#pragma unroll
                for (int i = 0; i < 8; ++i) st16(c, rb + i * 4096, z);
            }
        }
        __syncthreads();
#define WAITQ(NA, NB, mt) asm volatile("s_cmp_lt_u32 %8, 4\n\ts_cbranch_scc1 1f\n\ts_waitcnt vmcnt(" #NB ")\n1:\n\ts_waitcnt vmcnt(" #NA ")" \
            : "+v"(aq[mt][0]), "+v"(aq[mt][1]), "+v"(aq[mt][2]), "+v"(aq[mt][3]), "+v"(aq[mt + 1][0]), "+v"(aq[mt + 1][1]), "+v"(aq[mt + 1][2]), "+v"(aq[mt + 1][3]) : "s"(w) : "memory", "scc")
        const int fs = swz16(n);
#pragma unroll
        for (int ks = 0; ks < 2; ++ks) {
            if (ks == 0) {
                WAITQ(40, 12, 0);
                asm volatile("" : "+v"(xr0), "+v"(xr1), "+v"(xr2), "+v"(cwarm));
            } else WAITQ(32, 0, 2);
#pragma unroll
            for (int ct = 0; ct < 4; ++ct) {
                f32x4 a0 = Z4, a1 = Z4;
#pragma unroll
                for (int s = 0; s < 4; ++s) {
                    const v4u bu = *(LAS v4u*)(L + L_QA + (16 * ct + n) * 256 + (((4 * s + g) ^ fs) * 16));
                    a0 = mfma(aq[2 * ks][s], bu, a0); a1 = mfma(aq[2 * ks + 1][s], bu, a1);
                }
                wqk[ks][ct] = v4u{pack2(a0.x, a0.y), pack2(a0.z, a0.w), pack2(a1.x, a1.y), pack2(a1.z, a1.w)};
                pin(wqk[ks][ct]);
            }
        }
    }
    STAMP(1);
    const float xin0 = bflo(xr0), xin1 = bflo(xr1);
    float xres = bflo(xr2);
    unsigned warm = 0;

    auto layer_body = [&](auto LC) {
        constexpr int layer = decltype(LC)::value;
        constexpr unsigned T = layer == 0 ? 1u : 0u;
        {
            IDS();
            float x0 = xin0, x1 = xin1;
            if constexpr (layer == 1) {
#pragma unroll
                for (int ks = 0; ks < 2; ++ks)
#pragma unroll
                    for (int ct = 0; ct < 4; ++ct) wqk[ks][ct] = *(LAS v4u*)(L + w * 16384 + 8192 + (ks * 4 + ct) * 1024 + lane * 16);
                nap(isRed ? D_E6R : (isGU ? D_E6G : D_E6));
                int spins = 0;
                for (;;) {
                    const v2u v = ld8(c, WS_E6 + tid * 8);
                    x0 = u2f(v.x); x1 = u2f(v.y);
                    if (__all(ok2(v, 1u))) break;
                    if (give_up(c, spins)) break;
                }
                STAMP(20);
            }
            v4u xf[2];
            ln_frags(x0, x1, L, w, lane, xf);
            LAS float* rd = (LAS float*)(L + L_RED);
#pragma unroll
            for (int ct = 0; ct < 4; ++ct) {
                f32x4 a = mfma(xf[0], wqk[0][ct], Z4);
                a = mfma(xf[1], wqk[1][ct], a);
                if (g == 0) { rd[w * 128 + 16 * ct + n] = a.x + a.z; rd[w * 128 + 64 + 16 * ct + n] = a.y + a.w; }
            }
            __syncthreads();
            if (tid < 64) {
                float v0 = 0.f, v1 = 0.f;
#pragma unroll
                for (int i = 0; i < 8; ++i) { const v2u t = *(LAS v2u*)(rd + i * 128 + 2 * tid); v0 += u2f(t.x); v1 += u2f(t.y); }
                const float SC = 0.08838834764831845f * 1.4426950408889634f;
                const int b = tid >> 5;
                st4(c, WS_E1 + ((b * 2 + (h1 & 1)) * 16 + (h1 >> 1)) * 1024 + (64 * j1 + 2 * (tid & 31)) * 2, pack2_tag(v0 * SC, v1 * SC, T));
            }
            STAMP(2 + 20 * layer);
        }
        {
            IDS();
            if constexpr (layer == 0) {
                const u16* pa = (tid & 128) ? p.wuv + (size_t)(64 * j1 + ((tid & 127) >> 1)) * HD + h1 * 128 + (tid & 1) * 64
                                            : p.wo + (size_t)h1 * 128 * MD + (size_t)j1 * 8192 + (tid & 127) * 64;
                if (w >= 4) asm volatile("global_load_dword %0, %1, off" : "=v"(warm) : "v"(pa) : "memory");
            }
            if (w >= 4) {
                const int t = tid - 256;
                v4u qg[4];
                const int e1 = WS_E1 + (bb * 2 + hg) * 16384 + t * 16;
                if constexpr (layer == 0) {
                    nap(D_E1A);
                } else nap(D_E1);
                int spins = 0;
                for (;;) {
#pragma unroll
                    for (int k = 0; k < 4; ++k) qg[k] = ld16(c, e1 + k * 4096);
                    if (__all(ok4(qg[0], T) & ok4(qg[1], T) & ok4(qg[2], T) & ok4(qg[3], T))) break;
                    if (give_up(c, spins)) break;
                }
#pragma unroll
                for (int k = 0; k < 4; ++k) { const int u = t + 256 * k; *(LAS v4u*)(L + L_QA + (u >> 6) * 1040 + (u & 63) * 16) = qg[k]; }
            } else if constexpr (layer == 0) {
                asm volatile("s_waitcnt vmcnt(0)" ::: "memory");
            }
            __syncthreads();
            STAMP(3 + 20 * layer);
            if constexpr (layer == 0) {
                const int fs = swz16(n);
#pragma unroll
                for (int s = 0; s < 16; ++s) ck[s] = *(LAS v4u*)(L + (16 * w + n) * 1024 + (((4 * s + g) ^ fs) * 16));
                const int q = n >> 2, pp = n & 3;
#pragma unroll
                for (int ct = 0; ct < 4; ++ct) {
                    unsigned ad[8];
#pragma unroll
                    for (int s = 0; s < 4; ++s)
#pragma unroll
                        for (int t = 0; t < 2; ++t) {
                            const int r = 32 * s + 8 * g + 4 * t + q;
                            ad[2 * s + t] = lds0 + r * 1024 + (((8 * w + 2 * ct + (pp >> 1)) ^ swz16(r & 15)) * 16) + 8 * (pp & 1);
                        }
                    tr8(ckT[ct], ad);
                }
#pragma unroll
                for (int s = 0; s < 16; ++s) pin(ck[s]);
                __syncthreads();
                const int rr = lane >> 3;
#pragma unroll 4
                for (int i = 0; i < 16; ++i) {
                    const int d = 8 * i + rr;
                    const int cs = (lane & 7) ^ (((d >> 1) & 1) << 1) ^ (((d >> 3) & 1) << 2);
                    glds16(p.wo + (size_t)(h1 * 128 + d) * MD + 64 * w + cs * 8, lds0 + w * 16384 + i * 1024);
                }
            }
        }
        {
            IDS();
            f32x4 s0 = Z4, s1 = Z4;
#pragma unroll
            for (int s = 0; s < 16; s += 2) {
                const v4u q0 = *(LAS v4u*)(L + L_QA + n * 1040 + 64 * s + 16 * g);
                const v4u q1 = *(LAS v4u*)(L + L_QA + n * 1040 + 64 * (s + 1) + 16 * g);
                s0 = mfma(ck[s], q0, s0); s1 = mfma(ck[s + 1], q1, s1);
            }
            s0 += s1;
            float mx = fmaxf(fmaxf(s0.x, s0.y), fmaxf(s0.z, s0.w));
            mx = x32_max(x16_max(mx));
            LAS float* smax = (LAS float*)(L + L_SMAX); LAS float* ssum = (LAS float*)(L + L_SSUM);
            if (g == 0) smax[w * 16 + n] = mx;
            __syncthreads();
            float M = smax[n];
#pragma unroll
            for (int i = 1; i < 8; ++i) M = fmaxf(M, smax[i * 16 + n]);
            const unsigned p01 = pack2(__builtin_amdgcn_exp2f(s0.x - M), __builtin_amdgcn_exp2f(s0.y - M));
            const unsigned p23 = pack2(__builtin_amdgcn_exp2f(s0.z - M), __builtin_amdgcn_exp2f(s0.w - M));
            float ps = (bflo(p01) + bfhi(p01)) + (bflo(p23) + bfhi(p23));
            ps = x32_sum(x16_sum(ps));
            if (g == 0) ssum[w * 16 + n] = ps;
            *(LAS v2u*)(L + L_P + n * 272 + (16 * w + 4 * g) * 2) = v2u{p01, p23};
            if constexpr (layer == 0) {
#pragma unroll
                for (int i = 0; i < 2; ++i) {
                    const int r = 8 * w + 4 * i + g;
                    glds16(p.wuv + (size_t)(64 * j1 + r) * HD + h1 * 128 + ((n ^ swz16(r & 15)) * 8), lds0 + L_QA + (8 * w + 4 * i) * 256);
                }
            }
            __syncthreads();
            v4u pf[4];
#pragma unroll
            for (int s = 0; s < 4; ++s) pf[s] = *(LAS v4u*)(L + L_P + n * 272 + 64 * s + 16 * g);
            float l = ssum[n];
#pragma unroll
            for (int i = 1; i < 8; ++i) l += ssum[i * 16 + n];
            const float inv = 1.0f / l;
            f32x4 o[4];
#pragma unroll
            for (int ct = 0; ct < 4; ++ct) {
                f32x4 a = Z4;
#pragma unroll
                for (int s = 0; s < 4; ++s) a = mfma(ckT[ct][s], pf[s], a);
                o[ct] = a * inv;
            }
            const int h = 2 * n + hg;
            const int dst = WS_E2 + (h * 8 + w) * 16384 + (bb * 64 + split) * 128 + 16 * g;
            st16(c, dst, v4u{pack2_tag(o[0].x, o[0].y, T), pack2_tag(o[0].z, o[0].w, T), pack2_tag(o[1].x, o[1].y, T), pack2_tag(o[1].z, o[1].w, T)});
            st16(c, dst + 64, v4u{pack2_tag(o[2].x, o[2].y, T), pack2_tag(o[2].z, o[2].w, T), pack2_tag(o[3].x, o[3].y, T), pack2_tag(o[3].z, o[3].w, T)});
            if (w == 0 && g == 0) st4(c, WS_ST + ((bb * 32 + h) * 64 + split) * 4, tagf(M + __builtin_amdgcn_logf(l), T));
            STAMP(4 + 20 * layer);
        }
        v4u wvo[2][4];
        if constexpr (layer == 0) {
            IDS();
            asm volatile("s_waitcnt vmcnt(0)" ::: "memory");
            asm volatile("" ::"v"(warm));
            v4u bo[4][4];
            const int q = n >> 2, pp = n & 3;
#pragma unroll
            for (int nt = 0; nt < 4; ++nt) {
                unsigned ad[8];
#pragma unroll
                for (int s = 0; s < 4; ++s)
#pragma unroll
                    for (int t = 0; t < 2; ++t) {
                        const int d = 32 * s + 8 * g + 4 * t + q;
                        const int cs = (2 * nt + (pp >> 1)) ^ (((d >> 1) & 1) << 1) ^ (((d >> 3) & 1) << 2);
                        ad[2 * s + t] = lds0 + w * 16384 + d * 128 + cs * 16 + 8 * (pp & 1);
                    }
                tr8(bo[nt], ad);
            }
#pragma unroll
            for (int ks = 0; ks < 2; ++ks)
#pragma unroll
                for (int ct = 0; ct < 4; ++ct) *(LAS v4u*)(L + w * 16384 + 8192 + (ks * 4 + ct) * 1024 + lane * 16) = wqk[ks][ct];
            __syncthreads();
            const int fs = swz16(n);
#pragma unroll
            for (int ks = 0; ks < 2; ++ks) {
                v4u u0[4], u1[4];
#pragma unroll
                for (int s = 0; s < 4; ++s) {
                    u0[s] = *(LAS v4u*)(L + L_QA + (32 * ks + n) * 256 + (((4 * s + g) ^ fs) * 16));
                    u1[s] = *(LAS v4u*)(L + L_QA + (32 * ks + 16 + n) * 256 + (((4 * s + g) ^ fs) * 16));
                }
#pragma unroll
                for (int nt = 0; nt < 4; ++nt) {
                    f32x4 a0 = Z4, a1 = Z4;
#pragma unroll
                    for (int s = 0; s < 4; ++s) { a0 = mfma(u0[s], bo[nt][s], a0); a1 = mfma(u1[s], bo[nt][s], a1); }
                    *(LAS v4u*)(L + w * 16384 + (ks * 4 + nt) * 1024 + lane * 16) =
                        v4u{pack2(a0.x, a0.y), pack2(a0.z, a0.w), pack2(a1.x, a1.y), pack2(a1.z, a1.w)};
                }
                FENCE();
            }
        }
        STAMP(5 + 20 * layer);
        {
            IDS();
#pragma unroll
            for (int ks = 0; ks < 2; ++ks)
#pragma unroll
                for (int nt = 0; nt < 4; ++nt) wvo[ks][nt] = *(LAS v4u*)(L + w * 16384 + (ks * 4 + nt) * 1024 + lane * 16);
            if constexpr (layer == 1) nap(D_E2);
            const int b = w >> 2, chunk = lane & 7, sp0 = (tid >> 3) & 31;
            const int ib = WS_E2 + (h1 * 8 + j1) * 16384 + (b * 64 + sp0) * 128 + chunk * 16;
            const int sb = WS_ST + ((b * 32 + h1) * 64 + lane) * 4;
            v4u d0, d1; unsigned ls;
            int spins = 0;
            for (;;) {
                d0 = ld16(c, ib); d1 = ld16(c, ib + 32 * 128); ls = ld4(c, sb);
                if (__all(ok4(d0, T) & ok4(d1, T) & (((ls ^ T) & 1u) == 0u))) break;
                if (give_up(c, spins)) break;
            }
            STAMP(6 + 20 * layer);
            const float lse = u2f(ls);
            const float Mx = wave_max(lse);
            const float e = __builtin_amdgcn_exp2f(lse - Mx);
            const float wn = e / wave_sum(e);
            const float w0 = __shfl(wn, sp0), w1 = __shfl(wn, sp0 + 32);
            float a[8];
            a[0] = w0 * bflo(d0.x) + w1 * bflo(d1.x); a[1] = w0 * bfhi(d0.x) + w1 * bfhi(d1.x);
            a[2] = w0 * bflo(d0.y) + w1 * bflo(d1.y); a[3] = w0 * bfhi(d0.y) + w1 * bfhi(d1.y);
            a[4] = w0 * bflo(d0.z) + w1 * bflo(d1.z); a[5] = w0 * bfhi(d0.z) + w1 * bfhi(d1.z);
            a[6] = w0 * bflo(d0.w) + w1 * bflo(d1.w); a[7] = w0 * bfhi(d0.w) + w1 * bfhi(d1.w);
#pragma unroll
            for (int i = 0; i < 8; ++i) { a[i] += dpp<0x128>(a[i]); a[i] = x32_sum(x16_sum(a[i])); }
            LAS float* rd = (LAS float*)(L + L_RED);
            if (lane < 8) {
                *(LAS f32x4*)(rd + w * 64 + lane * 8) = f32x4{a[0], a[1], a[2], a[3]};
                *(LAS f32x4*)(rd + w * 64 + lane * 8 + 4) = f32x4{a[4], a[5], a[6], a[7]};
            }
            __syncthreads();
            if (tid < 128) {
                const int bq = tid >> 6, pos = tid & 63;
                const float v = (rd[(4 * bq) * 64 + pos] + rd[(4 * bq + 1) * 64 + pos]) + (rd[(4 * bq + 2) * 64 + pos] + rd[(4 * bq + 3) * 64 + pos]);
                const unsigned hi = pack2(v, 0.f);
                const unsigned lo = pack2(v - bflo(hi), 0.f);
                LAS u16* x2 = (LAS u16*)(L + L_X2);
                x2[bq * 64 + pos] = (u16)hi; x2[(2 + bq) * 64 + pos] = (u16)lo;
            }
            __syncthreads();
            v4u of[2];
#pragma unroll
            for (int ks = 0; ks < 2; ++ks) { v4u v = *(LAS v4u*)(L + L_X2 + (n & 3) * 128 + 64 * ks + 16 * g); if (n >= 4) v = v4u{0u, 0u, 0u, 0u}; of[ks] = v; }
#pragma unroll
            for (int nt = 0; nt < 4; ++nt) {
                f32x4 a2 = mfma(of[0], wvo[0][nt], Z4);
                a2 = mfma(of[1], wvo[1][nt], a2);
                if (g == 0) st8(c, WS_E3 + (4 * w + nt) * 32768 + cu * 128 + n * 8, v2u{tagf(a2.x + a2.z, T), tagf(a2.y + a2.w, T)});
            }
            STAMP(7 + 20 * layer);
        }
        if (isRed) {
            IDS();
            nap(D_E3);
            const int ib = WS_E3 + red * 32768 + tid * 16;
            v4u d[4];
            int spins = 0;
            for (;;) {
#pragma unroll
                for (int k = 0; k < 4; ++k) d[k] = ld16(c, ib + k * 8192);
                if (__all(ok4(d[0], T) & ok4(d[1], T) & ok4(d[2], T) & ok4(d[3], T))) break;
                if (give_up(c, spins)) break;
            }
            STAMP(8 + 20 * layer);
            float a[4];
            a[0] = (u2f(d[0].x) + u2f(d[1].x)) + (u2f(d[2].x) + u2f(d[3].x));
            a[1] = (u2f(d[0].y) + u2f(d[1].y)) + (u2f(d[2].y) + u2f(d[3].y));
            a[2] = (u2f(d[0].z) + u2f(d[1].z)) + (u2f(d[2].z) + u2f(d[3].z));
            a[3] = (u2f(d[0].w) + u2f(d[1].w)) + (u2f(d[2].w) + u2f(d[3].w));
#pragma unroll
            for (int i = 0; i < 4; ++i) { a[i] += dpp<0x128>(a[i]); a[i] = x32_sum(x16_sum(a[i])); }
            LAS float* rd = (LAS float*)(L + L_RED);
            if (lane < 8) *(LAS f32x4*)(rd + w * 32 + lane * 4) = f32x4{a[0], a[1], a[2], a[3]};
            __syncthreads();
            if (tid < 32) {
                float v = 0.f;
#pragma unroll
                for (int i = 0; i < 8; ++i) v += rd[i * 32 + tid];
                xres += v;
                st4(c, WS_E4 + red * 128 + tid * 4, tagf(xres, T));
            }
            STAMP(9 + 20 * layer);
            if constexpr (layer == 0) {
                const u16* pw = p.w2 + (size_t)(224 * w + n) * MD + 32 * (red >> 1) + 8 * g;
                const v4u Isel = (red & 1) ? Ihi : Ilo;
                v4u raw[14];
#pragma unroll
                for (int ft = 0; ft < 14; ++ft) raw[ft] = ldnt(pw + (size_t)ft * 16 * MD);
                FENCE();
#pragma unroll
                for (int ks = 0; ks < 7; ++ks) {
                    const f32x4 a0 = mfma(raw[2 * ks], Isel, Z4), a1 = mfma(raw[2 * ks + 1], Isel, Z4);
                    glf[ks] = v4u{pack2(a0.x, a0.y), pack2(a0.z, a0.w), pack2(a1.x, a1.y), pack2(a1.z, a1.w)};
                    pin(glf[ks]);
                }
            }
        }
        if (isGU) {
            IDS();
            if constexpr (layer == 0) {
                v4u raw[2][4];
#pragma unroll
                for (int mt = 0; mt < 4; ++mt) {
                    raw[0][mt] = ldnt(p.w1 + (size_t)(64 * w + 16 * mt + n) * FF + 32 * gu + 8 * g);
                    raw[1][mt] = ldnt(p.w3 + (size_t)(64 * w + 16 * mt + n) * FF + 32 * gu + 8 * g);
                }
                FENCE();
#pragma unroll
                for (int mat = 0; mat < 2; ++mat)
#pragma unroll
                    for (int ks = 0; ks < 2; ++ks) {
                        const f32x4 l0 = mfma(raw[mat][2 * ks], Ilo, Z4), h0 = mfma(raw[mat][2 * ks], Ihi, Z4);
                        const f32x4 l1 = mfma(raw[mat][2 * ks + 1], Ilo, Z4), hh1 = mfma(raw[mat][2 * ks + 1], Ihi, Z4);
                        glf[mat * 4 + 0 + ks] = v4u{pack2(l0.x, l0.y), pack2(l0.z, l0.w), pack2(l1.x, l1.y), pack2(l1.z, l1.w)};
                        glf[mat * 4 + 2 + ks] = v4u{pack2(h0.x, h0.y), pack2(h0.z, h0.w), pack2(hh1.x, hh1.y), pack2(hh1.z, hh1.w)};
                        pin(glf[mat * 4 + ks]); pin(glf[mat * 4 + 2 + ks]);
                    }
            } else nap(D_E4);
            float x0, x1;
            int spins = 0;
            for (;;) {
                const v2u v = ld8(c, WS_E4 + tid * 8);
                x0 = u2f(v.x); x1 = u2f(v.y);
                if (__all(ok2(v, T))) break;
                if (give_up(c, spins)) break;
            }
            STAMP(10 + 20 * layer);
            v4u xf[2];
            ln_frags(x0, x1, L, w, lane, xf);
            LAS float* rd = (LAS float*)(L + L_RED);
#pragma unroll
            for (int q = 0; q < 4; ++q) {
                f32x4 a = mfma(xf[0], glf[2 * q], Z4);
                a = mfma(xf[1], glf[2 * q + 1], a);
                if (g == 0) *(LAS v2u*)(rd + w * 128 + (16 * q + n) * 2) = v2u{f2u(a.x + a.z), f2u(a.y + a.w)};
            }
            __syncthreads();
            if (tid < 64) {
                float gt = 0.f, up = 0.f;
#pragma unroll
                for (int i = 0; i < 8; ++i) { gt += rd[i * 128 + tid]; up += rd[i * 128 + 64 + tid]; }
                const float hv = gt / (1.0f + __expf(-gt)) * up;
                st4(c, WS_E5 + gu * 256 + tid * 4, tagf(hv, T));
            }
            STAMP(11 + 20 * layer);
        }
        if (isRed) {
            IDS();
            if constexpr (layer == 1) nap(D_E5);
            const int ib = WS_E5 + (112 * w + lane) * 16;
            v4u d0, d1;
            int spins = 0;
            for (;;) {
                d0 = ld16(c, ib);
                d1 = (lane < 48) ? ld16(c, ib + 1024) : d0;
                if (__all(ok4(d0, T) & ok4(d1, T))) break;
                if (give_up(c, spins)) break;
            }
            STAMP(12 + 20 * layer);
            LAS unsigned char* hs = L + L_QA + w * 1856;
#pragma unroll
            for (int k = 0; k < 2; ++k) {
                const v4u d = k ? d1 : d0;
                const int f = 2 * (lane + 64 * k);
                const int pos = (f & ~31) + 8 * ((f >> 2) & 3) + 4 * ((f >> 4) & 1) + (f & 3);
                const unsigned h0 = pack2(u2f(d.x), u2f(d.z)), hb1 = pack2(u2f(d.y), u2f(d.w));
                const unsigned l0 = pack2(u2f(d.x) - bflo(h0), u2f(d.z) - bfhi(h0)), lb1 = pack2(u2f(d.y) - bflo(hb1), u2f(d.w) - bfhi(hb1));
                if (k == 0 || lane < 48) {
                    *(LAS unsigned*)(hs + pos * 2) = h0; *(LAS unsigned*)(hs + 464 + pos * 2) = hb1;
                    *(LAS unsigned*)(hs + 928 + pos * 2) = l0; *(LAS unsigned*)(hs + 1392 + pos * 2) = lb1;
                }
            }
            __builtin_amdgcn_wave_barrier();
            f32x4 a = Z4;
#pragma unroll
            for (int ks = 0; ks < 7; ++ks) {
                v4u v = *(LAS v4u*)(hs + (n & 3) * 464 + 64 * ks + 16 * g);
                if (n >= 4) v = v4u{0u, 0u, 0u, 0u};
                a = mfma(v, glf[ks], a);
            }
            LAS float* rd = (LAS float*)(L + L_RED);
            if (g == 0) *(LAS v2u*)(rd + w * 32 + n * 2) = v2u{f2u(a.x + a.z), f2u(a.y + a.w)};
            __syncthreads();
            if (tid < 32) {
                float v = 0.f;
#pragma unroll
                for (int i = 0; i < 8; ++i) v += rd[i * 32 + tid];
                xres += v;
                if constexpr (layer == 0) st4(c, WS_E6 + red * 128 + tid * 4, tagf(xres, 1u));
                else p.out[(tid & 1) * MD + 16 * red + (tid >> 1)] = (u16)pack2(xres, 0.f);
            }
            STAMP(13 + 20 * layer);
        }
    };
    layer_body(std::integral_constant<int, 0>{});
    layer_body(std::integral_constant<int, 1>{});
    asm volatile("mk_code_end:");
}
}

extern "C" void kernel_launch(void* const* d_in, const int* in_sizes, int n_in,
                              void* d_out, int out_size, void* d_ws, size_t ws_size,
                              hipStream_t stream) {
    mk::Params p;
    p.x = (const u16*)d_in[0]; p.ckv = (const u16*)d_in[1]; p.wq = (const u16*)d_in[2]; p.wuk = (const u16*)d_in[3];
    p.wuv = (const u16*)d_in[4]; p.wo = (const u16*)d_in[5]; p.w1 = (const u16*)d_in[7]; p.w3 = (const u16*)d_in[8];
    p.w2 = (const u16*)d_in[9]; p.out = (u16*)d_out; p.ws = (unsigned char*)d_ws;
    (void)hipMemsetAsync(d_ws, 0, mk::WS_ZERO_BYTES, stream);
    hipLaunchKernelGGL(mk::mega, dim3(mk::NWG), dim3(mk::NT), 0, stream, p);
}
```

```cpp
#include <hip/hip_runtime.h>
#include <hip/hip_bf16.h>
#include <type_traits>

typedef unsigned short u16;
typedef unsigned v4u __attribute__((ext_vector_type(4)));
typedef unsigned v2u __attribute__((ext_vector_type(2)));
typedef float f32x4 __attribute__((ext_vector_type(4)));
typedef float f32x2 __attribute__((ext_vector_type(2)));
typedef short bf16x8 __attribute__((ext_vector_type(8)));
typedef __bf16 bf16x2 __attribute__((ext_vector_type(2)));
#define GAS __attribute__((address_space(1)))
#define LAS __attribute__((address_space(3)))
#define DI __device__ __forceinline__
#define RLX_AGENT __ATOMIC_RELAXED, __HIP_MEMORY_SCOPE_AGENT

#define STAMPS 0
#define WARMPOL "nt"
#define CKV_V 3
#define PROF 0

namespace mk {
constexpr int NWG = 256, NT = 512;
constexpr int SK = 8192, DC = 512, MD = 512, HD = 4096, FF = 1792;
constexpr int WS_CTRL = 0;
constexpr int WS_E1 = 256;
constexpr int WS_ST = WS_E1 + 65536;
constexpr int WS_E4 = WS_ST + 32768;
constexpr int WS_E5 = WS_E4 + 4096;
constexpr int WS_E6 = WS_E5 + 14336;
constexpr int WS_ZERO_BYTES = WS_E6 + 4096;
constexpr int WS_E2 = 131072;
constexpr int WS_E3 = WS_E2 + 256 * 16384;
constexpr int WS_STAMP = WS_E3 + 32 * 32768;
constexpr int WS_BYTES = WS_STAMP + 256 * 64 * 8;
static_assert(WS_ZERO_BYTES <= WS_E2 && (WS_ZERO_BYTES % 16) == 0, "ws map");
constexpr int L_A = 131072;
constexpr int L_TQ = L_A + 16384;
constexpr int L_RED = L_A + 17408;
constexpr int L_P = L_RED;
constexpr int L_SMAX = L_RED + 6400;
constexpr int L_XS = L_SMAX + 512;
constexpr int L_X2 = L_XS + 4096;
constexpr int L_BYTES = 163840;
static_assert(L_X2 + 512 <= L_BYTES, "LDS");

struct Params { const u16 *x, *ckv, *wq, *wuk, *wuv, *wo, *w1, *w3, *w2; u16* out; unsigned char* ws; };

DI float u2f(unsigned u) { return __builtin_bit_cast(float, u); }
DI unsigned f2u(float f) { return __builtin_bit_cast(unsigned, f); }
DI f32x4 mfma(v4u a, v4u b, f32x4 c) {
    return __builtin_amdgcn_mfma_f32_16x16x32_bf16(__builtin_bit_cast(bf16x8, a), __builtin_bit_cast(bf16x8, b), c, 0, 0, 0);
}
DI unsigned pack2(float a, float b) { bf16x2 v = {(__bf16)a, (__bf16)b}; return __builtin_bit_cast(unsigned, v); }
DI float bflo(unsigned u) { return u2f(u << 16); }
DI float bfhi(unsigned u) { return u2f(u & 0xffff0000u); }
template <unsigned T> DI unsigned pack2_tag(float a, float b) {
    const unsigned u = f2u(a);
    const unsigned lo = T ? ((u >> 16) | 1u) : (((u + 0x10000u) >> 17) << 1);
    return __builtin_amdgcn_perm(pack2(b, b), lo, 0x07060100u);
}
template <unsigned T> DI unsigned tagf(float a) { return T ? (f2u(a) | 1u) : (f2u(a) & ~1u); }
DI v4u ldnt(const u16* p) { return __builtin_nontemporal_load((const GAS v4u*)p); }
DI void glds16(const void* gsrc, unsigned lds_dst) {
    unsigned keep;
    asm volatile("s_mov_b32 %0, m0\n\ts_mov_b32 m0, %2\n\ts_nop 0\n\tglobal_load_lds_dwordx4 %1, off nt\n\ts_mov_b32 m0, %0" : "=&s"(keep) : "v"(gsrc), "s"(lds_dst) : "memory");
}
#define LDNT_ASM(dst, ptr) asm volatile("global_load_dwordx4 %0, %1, off nt" : "=v"(dst) : "v"(ptr) : "memory")
DI int swz16(int r) { return ((r >> 3) & 1) | ((r & 3) << 1) | ((((r >> 2) ^ (r >> 3)) & 1) << 3); }
DI int swz8(int r) { return (r & 2) | ((r >> 1) & 4); }
#define TR_ISSUE(dst, addr, OFF) asm volatile("ds_read_b64_tr_b16 %0, %1 offset:" #OFF : "=v"(dst) : "v"(addr) : "memory")

template <int CTRL> DI unsigned dppu(unsigned v) { return (unsigned)__builtin_amdgcn_update_dpp(0, (int)v, CTRL, 0xf, 0xf, true); }
template <int CTRL> DI float dpp(float v) { return u2f(dppu<CTRL>(f2u(v))); }
DI float row_sum16(float v) { v += dpp<0xB1>(v); v += dpp<0x4E>(v); v += dpp<0x141>(v); v += dpp<0x140>(v); return v; }
DI float row_max16(float v) { v = fmaxf(v, dpp<0xB1>(v)); v = fmaxf(v, dpp<0x4E>(v)); v = fmaxf(v, dpp<0x141>(v)); v = fmaxf(v, dpp<0x140>(v)); return v; }
DI float x16_sum(float v) { auto r = __builtin_amdgcn_permlane16_swap(f2u(v), f2u(v), false, false); return u2f(r[0]) + u2f(r[1]); }
DI float x32_sum(float v) { auto r = __builtin_amdgcn_permlane32_swap(f2u(v), f2u(v), false, false); return u2f(r[0]) + u2f(r[1]); }
DI float x16_max(float v) { auto r = __builtin_amdgcn_permlane16_swap(f2u(v), f2u(v), false, false); return fmaxf(u2f(r[0]), u2f(r[1])); }
DI float x32_max(float v) { auto r = __builtin_amdgcn_permlane32_swap(f2u(v), f2u(v), false, false); return fmaxf(u2f(r[0]), u2f(r[1])); }
DI float wave_sum(float v) { return x32_sum(x16_sum(row_sum16(v))); }
DI float wave_max(float v) { return x32_max(x16_max(row_max16(v))); }
DI void pin(v4u& v) { asm volatile("" : "+v"(v)); }
DI void nap(int n) {
#pragma unroll 1
    for (int i = 0; i < n; ++i) __builtin_amdgcn_s_sleep(1);
}

struct Ctx {
    __amdgpu_buffer_rsrc_t rs;
    GAS unsigned* abortw;
    unsigned long long t0;
    bool dead;
};
DI v4u ld16(const Ctx& c, int off) { return __builtin_amdgcn_raw_buffer_load_b128(c.rs, off, 0, 16); }
DI v2u ld8(const Ctx& c, int off) { return __builtin_amdgcn_raw_buffer_load_b64(c.rs, off, 0, 16); }
DI void st16(const Ctx& c, int off, v4u v) { __builtin_amdgcn_raw_buffer_store_b128(v, c.rs, off, 0, 16); }
DI void st8(const Ctx& c, int off, v2u v) { __builtin_amdgcn_raw_buffer_store_b64(v, c.rs, off, 0, 16); }
DI void st4(const Ctx& c, int off, unsigned v) { __builtin_amdgcn_raw_buffer_store_b32(v, c.rs, off, 0, 16); }
DI bool give_up(Ctx& c, int& spins) {
    if (c.dead) return true;
    if ((++spins & 63) == 0) {
        const bool late = (long long)(__builtin_amdgcn_s_memrealtime() - c.t0) > 5000000ll;
        if (late || __hip_atomic_load(c.abortw, RLX_AGENT) != 0u) { __hip_atomic_store(c.abortw, 1u, RLX_AGENT); c.dead = true; return true; }
    }
    return false;
}
template <unsigned T> DI unsigned andor(unsigned a, unsigned b) { return T ? (a & b) : (a | b); }
template <unsigned T> DI unsigned v4(v4u v) { return andor<T>(andor<T>(v.x, v.y), andor<T>(v.z, v.w)); }
template <unsigned T> DI bool okbit(unsigned m) { return (m & 1u) == T; }

template <bool PERM> DI void mk_frags(float v0, float v1, LAS unsigned char* xs, int lane, v4u (&fr)[2]) {
    const int n = lane & 15, g = lane >> 4;
    const unsigned hi = pack2(v0, v1);
    const unsigned lo = pack2(v0 - bflo(hi), v1 - bfhi(hi));
    const bool odd = lane & 1;
    const unsigned recv = dppu<0xB1>(odd ? hi : lo);
    const unsigned a = odd ? recv : hi, b = odd ? lo : recv;
    const unsigned w0 = __builtin_amdgcn_perm(b, a, 0x05040100u), w1 = __builtin_amdgcn_perm(b, a, 0x07060302u);
    const int pos2 = PERM ? (16 * (lane >> 5) + 4 * ((lane >> 2) & 3) + 2 * ((lane >> 4) & 1) + ((lane >> 1) & 1)) : (lane >> 1);
    LAS unsigned* x32 = (LAS unsigned*)xs + (odd ? 64 : 0) + pos2;
    x32[0] = w0; x32[32] = w1;
    const unsigned cst = n == 4 ? 0x3f803f80u : 0u;
#pragma unroll
    for (int ks = 0; ks < 2; ++ks) {
        v4u v = {cst, cst, cst, cst};
        if (n < 4) v = *(LAS v4u*)(xs + n * 128 + 64 * ks + 16 * g);
        fr[ks] = v;
    }
}

#define IDS() int tid = threadIdx.x; asm volatile("" : "+v"(tid)); const int lane = tid & 63, n = lane & 15, g = lane >> 4; (void)n; (void)g; (void)lane
#define FENCE() __builtin_amdgcn_sched_barrier(0)
#define STAMP(i) do {} while (0)

#define D_E1A 0
#define D_E1 24
#define D_E2 40
#define D_E3 20
#define D_E4 70
#define D_E5 50
#define D_E6 130
#define D_E6R 14
#define D_E6G 50

DI void ln_gemv_partials(float x0, float x1, const v4u (&bf)[2][4], LAS unsigned char* L, int w, int lane) {
    const int n = lane & 15, g = lane >> 4;
    v4u xf[2];
    mk_frags<true>(x0, x1, L + L_XS + w * 512, lane, xf);
    LAS unsigned char* rq = L + L_RED + w * 512 + n * 8;
    LAS unsigned char* rc = L + L_RED + 4096 + w * 256 + n * 4;
#pragma unroll
    for (int ct = 0; ct < 4; ++ct) {
        f32x4 a = mfma(xf[0], bf[0][ct], f32x4{0.f, 0.f, 0.f, 0.f});
        a = mfma(xf[1], bf[1][ct], a);
        if (g == 0) *(LAS f32x2*)(rq + ct * 128) = f32x2{a.x + a.z, a.y + a.w};
        if (g == 1) *(LAS float*)(rc + ct * 64) = a.x;
    }
    f32x4 gm = mfma(xf[0], xf[0], f32x4{0.f, 0.f, 0.f, 0.f});
    gm = mfma(xf[1], xf[1], gm);
    LAS float* rs = (LAS float*)(L + L_RED + 6144 + w * 16);
    if (lane == 4) *(LAS f32x2*)rs = f32x2{gm.x + gm.z, gm.y + gm.w};
    if (lane < 2) rs[2 + lane] = lane == 0 ? gm.x + 2.f * gm.z : gm.y + 2.f * gm.w;
}
DI void ln_gemv_final(LAS unsigned char* L, int t, float (&o)[4]) {
    f32x4 q = {0.f, 0.f, 0.f, 0.f}, st = {0.f, 0.f, 0.f, 0.f};
    f32x2 cs = {0.f, 0.f};
#pragma unroll
    for (int i = 0; i < 8; ++i) {
        q += *(LAS f32x4*)(L + L_RED + i * 512 + t * 16);
        cs += *(LAS f32x2*)(L + L_RED + 4096 + i * 256 + t * 8);
        st += *(LAS f32x4*)(L + L_RED + 6144 + i * 16);
    }
    const float m0 = st.x * (1.f / 512), m1 = st.y * (1.f / 512);
    const float r0 = __builtin_amdgcn_rsqf(fmaxf(st.z * (1.f / 512) - m0 * m0, 0.f) + 1e-5f);
    const float r1 = __builtin_amdgcn_rsqf(fmaxf(st.w * (1.f / 512) - m1 * m1, 0.f) + 1e-5f);
    o[0] = (q.x - m0 * cs.x) * r0; o[1] = (q.y - m1 * cs.x) * r1;
    o[2] = (q.z - m0 * cs.y) * r0; o[3] = (q.w - m1 * cs.y) * r1;
}

__global__ void __launch_bounds__(NT, 2) mega(Params p) {
    __shared__ __attribute__((aligned(16))) unsigned char smem[L_BYTES];
    LAS unsigned char* L = (LAS unsigned char*)smem;
    const int tid = threadIdx.x, lane = tid & 63, w = __builtin_amdgcn_readfirstlane(tid >> 6), n = lane & 15, g = lane >> 4;
    const int cu = blockIdx.x, xcd = cu & 7, idx = cu >> 3;
    const int h1 = xcd * 4 + (idx >> 3), j1 = idx & 7;
    const int gs = xcd * 16 + ((idx & 7) | ((idx >> 4) << 3)), bb = gs >> 6, split = gs & 63, hg = 1 - ((idx >> 3) & 1);
    const bool isRed = idx < 4, isGU = idx >= 8 && idx < 15;
    const int red = xcd * 4 + idx, gu = xcd * 7 + (idx - 8);
    unsigned cwarm = 0;
    {
        unsigned long long pc; unsigned len;
        asm volatile("mk_code_begin:\n\ts_getpc_b64 %0\n\ts_mov_b32 %1, mk_code_end-mk_code_begin" : "=s"(pc), "=s"(len));
        const unsigned long long ca = pc + (((unsigned)tid * 128u < len) ? (unsigned)tid * 128u : 0u);
        asm volatile("global_load_dword %0, %1, off" : "=v"(cwarm) : "v"(ca) : "memory");
    }
    Ctx c;
    c.rs = __builtin_amdgcn_make_buffer_rsrc((void*)p.ws, 0, WS_BYTES, 0x00020000);
    c.abortw = (GAS unsigned*)(p.ws + WS_CTRL);
    c.t0 = __builtin_amdgcn_s_memrealtime();
    c.dead = false;
    STAMP(0);
    const f32x4 Z4 = {0.f, 0.f, 0.f, 0.f};

    v4u ck[16];
    v4u ckT[4][4];
    v4u glf[2][4];
    const u16* ckv0 = p.ckv + ((size_t)bb * SK + (size_t)split * 128) * DC;
    unsigned xr0, xr1, xr2, xs0, xs1;
    {
        const u16* px = p.x + tid;
        const u16* pr = p.x + ((tid & 1) * MD + 16 * (red & 31) + ((tid >> 1) & 15));
        asm volatile("global_load_ushort %0, %5, off\n\tglobal_load_ushort %1, %5, off offset:1024\n\tglobal_load_ushort %2, %6, off\n\t"
                     "global_load_ushort %3, %7, off\n\tglobal_load_ushort %4, %7, off offset:1024"
                     : "=&v"(xr0), "=&v"(xr1), "=&v"(xr2), "=&v"(xs0), "=&v"(xs1) : "v"(px), "v"(pr), "v"(p.x) : "memory");
    }

    v4u wqk[2][4];
    const unsigned lds0 = (unsigned)(size_t)smem;
    auto ckv_rows = [&]() {
#pragma unroll 4
        for (int i = 0; i < 16; ++i) glds16(ckv0 + (size_t)(16 * w + i) * DC + ((lane ^ swz16(i)) * 8), lds0 + (16 * w + i) * 1024);
    };
    {
        v4u aq[4][4];
#pragma unroll
        for (int i = 0; i < 2; ++i) {
            const int r = 8 * w + 4 * i + g;
            glds16(p.wuk + (size_t)(64 * j1 + r) * HD + h1 * 128 + ((n ^ swz16(r & 15)) * 8), lds0 + L_A + (8 * w + 4 * i) * 256);
        }
        {
            const u16* pq = p.wq + (size_t)(64 * w + g) * HD + h1 * 128 + 8 * n;
#pragma unroll
            for (int mt = 0; mt < 4; ++mt)
#pragma unroll
                for (int i = 0; i < 4; ++i) { const u16* pp = pq + (size_t)(16 * mt + 4 * i) * HD; LDNT_ASM(aq[mt][i], pp); }
        }
        asm volatile("s_waitcnt vmcnt(16)" : "+v"(xr0), "+v"(xr1), "+v"(xr2), "+v"(xs0), "+v"(xs1), "+v"(cwarm) :: "memory");
        STAMP(14);
        __syncthreads();
        STAMP(15);
        const int fs = swz16(n);
        v4u bu[4][4];
#pragma unroll
        for (int ct = 0; ct < 4; ++ct)
#pragma unroll
            for (int s = 0; s < 4; ++s) bu[ct][s] = *(LAS v4u*)(L + L_A + (16 * ct + n) * 256 + (((4 * s + g) ^ fs) * 16));
#pragma unroll
        for (int ct = 0; ct < 4; ++ct)
#pragma unroll
            for (int s = 0; s < 4; ++s) pin(bu[ct][s]);
        __syncthreads();
        STAMP(16);
        if (w < 4) ckv_rows();
        else {
            const v4u z = {0u, 0u, 0u, 0u};
            const int t = tid - 256;
            const int ib = WS_E2 + (h1 * 8 + j1) * 16384 + t * 16;
#pragma unroll
            for (int i = 0; i < 4; ++i) st16(c, ib + i * 4096, z);
            st16(c, WS_E3 + cu * 4096 + t * 16, z);
        }
        STAMP(17);
#define WAITQ(NA, NB, mt) asm volatile("s_cmp_lt_u32 %4, 4\n\ts_cbranch_scc1 1f\n\ts_waitcnt vmcnt(" #NB ")\n1:\n\ts_waitcnt vmcnt(" #NA ")" \
            : "+v"(aq[mt][0]), "+v"(aq[mt][1]), "+v"(aq[mt][2]), "+v"(aq[mt][3]) : "s"(w) : "memory", "scc")
        LAS unsigned char* tq = L + L_A + w * 4096;
#pragma unroll
        for (int mt = 0; mt < 4; ++mt) {
            if (mt == 0) WAITQ(28, 17, 0);
            else if (mt == 1) WAITQ(24, 13, 1);
            else if (mt == 2) WAITQ(20, 9, 2);
            else { WAITQ(16, 0, 3); STAMP(18); }
#pragma unroll
            for (int i = 0; i < 4; ++i) { const int r = 4 * i + g; *(LAS v4u*)(tq + r * 256 + ((n ^ swz16(r)) * 16)) = aq[mt][i]; }
            v4u af[4];
#pragma unroll
            for (int s = 0; s < 4; ++s) af[s] = *(LAS v4u*)(tq + n * 256 + (((4 * s + g) ^ fs) * 16));
#pragma unroll
            for (int ct = 0; ct < 4; ++ct) {
                f32x4 a0 = Z4;
#pragma unroll
                for (int s = 0; s < 4; ++s) a0 = mfma(af[s], bu[ct][s], a0);
                if (mt & 1) { wqk[mt >> 1][ct].z = pack2(a0.x, a0.y); wqk[mt >> 1][ct].w = pack2(a0.z, a0.w); }
                else { wqk[mt >> 1][ct].x = pack2(a0.x, a0.y); wqk[mt >> 1][ct].y = pack2(a0.z, a0.w); }
            }
        }
        if (w >= 4) ckv_rows();
#pragma unroll
        for (int ks = 0; ks < 2; ++ks)
#pragma unroll
            for (int ct = 0; ct < 4; ++ct) pin(wqk[ks][ct]);
    }
    __syncthreads();
    STAMP(1);
    float xres = bflo(xr2);

    auto layer_body = [&](auto LC) {
        constexpr int layer = decltype(LC)::value;
        constexpr unsigned T = layer == 0 ? 1u : 0u;
        {
            IDS();
            float x0, x1;
            if constexpr (layer == 0) { x0 = bflo(xr0) - bflo(xs0); x1 = bflo(xr1) - bflo(xs1); }
            else {
#pragma unroll
                for (int ks = 0; ks < 2; ++ks)
#pragma unroll
                    for (int ct = 0; ct < 4; ++ct) wqk[ks][ct] = *(LAS v4u*)(L + w * 16384 + 8192 + (ks * 4 + ct) * 1024 + lane * 16);
                nap(isRed ? D_E6R : (isGU ? D_E6G : D_E6));
                int spins = 0;
                for (;;) {
                    const v2u v = ld8(c, WS_E6 + tid * 8), s = ld8(c, WS_E6);
                    x0 = u2f(v.x) - u2f(s.x); x1 = u2f(v.y) - u2f(s.y);
                    if (__all(okbit<1u>(v.x & v.y & s.x & s.y))) break;
                    if (give_up(c, spins)) break;
                }
                STAMP(20);
            }
            ln_gemv_partials(x0, x1, wqk, L, w, lane);
            __syncthreads();
            if (tid < 32) {
                float o[4];
                ln_gemv_final(L, tid, o);
                const float SC = 0.08838834764831845f * 1.4426950408889634f;
                const int dst = WS_E1 + ((h1 & 1) * 16 + (h1 >> 1)) * 1024 + (64 * j1 + 2 * tid) * 2;
                st4(c, dst, pack2_tag<T>(o[0] * SC, o[2] * SC));
                st4(c, dst + 32768, pack2_tag<T>(o[1] * SC, o[3] * SC));
            }
            STAMP(2 + 20 * layer);
        }
        v4u wuvr[2];
        {
            IDS();
            if constexpr (layer == 0) {
                {
                    const u16* pu = p.wuv + (size_t)(64 * j1 + (tid >> 4)) * HD + h1 * 128 + (tid & 15) * 8;
                    const u16* pu2 = pu + (size_t)32 * HD;
                    LDNT_ASM(wuvr[0], pu); LDNT_ASM(wuvr[1], pu2);
                }
                asm volatile("s_waitcnt vmcnt(2)" ::: "memory");
                STAMP(19);
                __syncthreads();
                const int fs = swz16(n);
#pragma unroll
                for (int s = 0; s < 16; ++s) ck[s] = *(LAS v4u*)(L + (16 * w + n) * 1024 + (((4 * s + g) ^ fs) * 16));
                v2u tr[4][8];
                const int q = n >> 2, pp = n & 3;
#pragma unroll
                for (int t = 0; t < 2; ++t) {
                    const int r = 8 * g + 4 * t + q;
#pragma unroll
                    for (int ct = 0; ct < 4; ++ct) {
                        const unsigned ad = lds0 + r * 1024 + (((8 * w + 2 * ct + (pp >> 1)) ^ swz16(r & 15)) * 16) + 8 * (pp & 1);
                        TR_ISSUE(tr[ct][0 + t], ad, 0); TR_ISSUE(tr[ct][2 + t], ad, 32768);
                        const unsigned ad2 = ad + 65536;
                        TR_ISSUE(tr[ct][4 + t], ad2, 0); TR_ISSUE(tr[ct][6 + t], ad2, 32768);
                    }
                }
                asm volatile("s_waitcnt lgkmcnt(0)" : "+v"(tr[0][0]), "+v"(tr[0][1]), "+v"(tr[0][2]), "+v"(tr[0][3]), "+v"(tr[0][4]), "+v"(tr[0][5]), "+v"(tr[0][6]), "+v"(tr[0][7]),
                             "+v"(tr[1][0]), "+v"(tr[1][1]), "+v"(tr[1][2]), "+v"(tr[1][3]), "+v"(tr[1][4]), "+v"(tr[1][5]), "+v"(tr[1][6]), "+v"(tr[1][7]) :: "memory");
                asm volatile("" : "+v"(tr[2][0]), "+v"(tr[2][1]), "+v"(tr[2][2]), "+v"(tr[2][3]), "+v"(tr[2][4]), "+v"(tr[2][5]), "+v"(tr[2][6]), "+v"(tr[2][7]),
                             "+v"(tr[3][0]), "+v"(tr[3][1]), "+v"(tr[3][2]), "+v"(tr[3][3]), "+v"(tr[3][4]), "+v"(tr[3][5]), "+v"(tr[3][6]), "+v"(tr[3][7]) :: "memory");
#pragma unroll
                for (int ct = 0; ct < 4; ++ct)
#pragma unroll
                    for (int s = 0; s < 4; ++s) ckT[ct][s] = v4u{tr[ct][2 * s].x, tr[ct][2 * s].y, tr[ct][2 * s + 1].x, tr[ct][2 * s + 1].y};
#pragma unroll
                for (int s = 0; s < 16; ++s) pin(ck[s]);
                __syncthreads();
                STAMP(17);
                if (w >= 4) {
                    const int rr = lane >> 3;
#pragma unroll 4
                    for (int i = 0; i < 32; ++i) {
                        const int v = (i & 1) ? w : w - 4, d = 8 * (i >> 1) + rr;
                        glds16(p.wo + (size_t)(h1 * 128 + d) * MD + 64 * v + (((lane & 7) ^ swz8(d)) * 8), lds0 + v * 16384 + (i >> 1) * 1024);
                    }
                }
            }
            if (w < 4) {
                const int t = tid;
                v4u qg[4];
                const int e1 = WS_E1 + (bb * 2 + hg) * 16384 + t * 16;
                nap(layer == 0 ? D_E1A : D_E1);
                int spins = 0;
                for (;;) {
#pragma unroll
                    for (int k = 0; k < 4; ++k) qg[k] = ld16(c, e1 + k * 4096);
                    if (__all(okbit<T>(andor<T>(andor<T>(v4<T>(qg[0]), v4<T>(qg[1])), andor<T>(v4<T>(qg[2]), v4<T>(qg[3])))))) break;
                    if (give_up(c, spins)) break;
                }
#pragma unroll
                for (int k = 0; k < 4; ++k) { const int u = t + 256 * k; *(LAS v4u*)(L + L_A + (u >> 6) * 1040 + (u & 63) * 16) = qg[k]; }
            }
            __syncthreads();
            STAMP(3 + 20 * layer);
        }
        {
            IDS();
            f32x4 s0 = Z4, s1 = Z4;
#pragma unroll
            for (int s = 0; s < 16; s += 2) {
                const v4u q0 = *(LAS v4u*)(L + L_A + n * 1040 + 64 * s + 16 * g);
                const v4u q1 = *(LAS v4u*)(L + L_A + n * 1040 + 64 * (s + 1) + 16 * g);
                s0 = mfma(ck[s], q0, s0); s1 = mfma(ck[s + 1], q1, s1);
            }
            s0 += s1;
            float mx = fmaxf(fmaxf(s0.x, s0.y), fmaxf(s0.z, s0.w));
            mx = x32_max(x16_max(mx));
            LAS float* smax = (LAS float*)(L + L_SMAX);
            if (g == 0) smax[n * 8 + w] = mx;
            __syncthreads();
            if constexpr (layer == 0) {
                asm volatile("s_cmp_lt_u32 %2, 4\n\ts_cbranch_scc1 1f\n\ts_waitcnt vmcnt(32)\n\ts_branch 2f\n1:\n\ts_waitcnt vmcnt(0)\n2:" : "+v"(wuvr[0]), "+v"(wuvr[1]) : "s"(w) : "memory", "scc");
                const int r = tid >> 4;
                LAS unsigned char* dst = L + L_A + r * 256 + (((tid & 15) ^ swz16(r & 15)) * 16);
                *(LAS v4u*)dst = wuvr[0]; *(LAS v4u*)(dst + 32 * 256) = wuvr[1];
            }
            const f32x4 ma = *(LAS f32x4*)(smax + n * 8), mb = *(LAS f32x4*)(smax + n * 8 + 4);
            const float M = fmaxf(fmaxf(fmaxf(ma.x, ma.y), fmaxf(ma.z, ma.w)), fmaxf(fmaxf(mb.x, mb.y), fmaxf(mb.z, mb.w)));
            const unsigned p01 = pack2(__builtin_amdgcn_exp2f(s0.x - M), __builtin_amdgcn_exp2f(s0.y - M));
            const unsigned p23 = pack2(__builtin_amdgcn_exp2f(s0.z - M), __builtin_amdgcn_exp2f(s0.w - M));
            *(LAS v2u*)(L + L_P + n * 272 + (16 * w + 4 * g) * 2) = v2u{p01, p23};
            __syncthreads();
            v4u pf[4];
#pragma unroll
            for (int s = 0; s < 4; ++s) pf[s] = *(LAS v4u*)(L + L_P + n * 272 + 64 * s + 16 * g);
            const int h = 2 * n + hg;
            const int dst = WS_E2 + (h * 8 + w) * 16384 + (bb * 64 + split) * 128 + 16 * g;
#pragma unroll
            for (int q = 0; q < 2; ++q) {
                f32x4 a = Z4, b = Z4;
#pragma unroll
                for (int s = 0; s < 4; ++s) { a = mfma(ckT[2 * q][s], pf[s], a); b = mfma(ckT[2 * q + 1][s], pf[s], b); }
                st16(c, dst + 64 * q, v4u{pack2_tag<T>(a.x, a.y), pack2_tag<T>(a.z, a.w), pack2_tag<T>(b.x, b.y), pack2_tag<T>(b.z, b.w)});
            }
            if (w == 0) {
                const v4u ones = {0x3f803f80u, 0x3f803f80u, 0x3f803f80u, 0x3f803f80u};
                f32x4 l = Z4;
#pragma unroll
                for (int s = 0; s < 4; ++s) l = mfma(ones, pf[s], l);
                if (g == 0) st8(c, WS_ST + ((bb * 32 + h) * 64 + split) * 8, v2u{tagf<T>(M), tagf<T>(l.x)});
            }
            STAMP(4 + 20 * layer);
        }
        v4u wvo[2][4];
        if constexpr (layer == 0) {
            IDS();
            asm volatile("s_waitcnt vmcnt(0)" ::: "memory");
            __syncthreads();
            v2u tr[4][8];
            const int q = n >> 2, pp = n & 3;
#pragma unroll
            for (int t = 0; t < 2; ++t) {
                const int d = 8 * g + 4 * t + q;
#pragma unroll
                for (int nt = 0; nt < 4; ++nt) {
                    const unsigned ad = lds0 + w * 16384 + d * 128 + (((2 * nt + (pp >> 1)) ^ swz8(d)) * 16) + 8 * (pp & 1);
                    TR_ISSUE(tr[nt][0 + t], ad, 0); TR_ISSUE(tr[nt][2 + t], ad, 4096); TR_ISSUE(tr[nt][4 + t], ad, 8192); TR_ISSUE(tr[nt][6 + t], ad, 12288);
                }
            }
            asm volatile("s_waitcnt lgkmcnt(0)" : "+v"(tr[0][0]), "+v"(tr[0][1]), "+v"(tr[0][2]), "+v"(tr[0][3]), "+v"(tr[0][4]), "+v"(tr[0][5]), "+v"(tr[0][6]), "+v"(tr[0][7]),
                         "+v"(tr[1][0]), "+v"(tr[1][1]), "+v"(tr[1][2]), "+v"(tr[1][3]), "+v"(tr[1][4]), "+v"(tr[1][5]), "+v"(tr[1][6]), "+v"(tr[1][7]) :: "memory");
            asm volatile("" : "+v"(tr[2][0]), "+v"(tr[2][1]), "+v"(tr[2][2]), "+v"(tr[2][3]), "+v"(tr[2][4]), "+v"(tr[2][5]), "+v"(tr[2][6]), "+v"(tr[2][7]),
                         "+v"(tr[3][0]), "+v"(tr[3][1]), "+v"(tr[3][2]), "+v"(tr[3][3]), "+v"(tr[3][4]), "+v"(tr[3][5]), "+v"(tr[3][6]), "+v"(tr[3][7]) :: "memory");
#pragma unroll
            for (int ks = 0; ks < 2; ++ks)
#pragma unroll
                for (int ct = 0; ct < 4; ++ct) *(LAS v4u*)(L + w * 16384 + 8192 + (ks * 4 + ct) * 1024 + lane * 16) = wqk[ks][ct];
            const int fs = swz16(n);
#pragma unroll
            for (int ks = 0; ks < 2; ++ks) {
                v4u u0[4], u1[4];
#pragma unroll
                for (int s = 0; s < 4; ++s) {
                    u0[s] = *(LAS v4u*)(L + L_A + (32 * ks + n) * 256 + (((4 * s + g) ^ fs) * 16));
                    u1[s] = *(LAS v4u*)(L + L_A + (32 * ks + 16 + n) * 256 + (((4 * s + g) ^ fs) * 16));
                }
#pragma unroll
                for (int nt = 0; nt < 4; ++nt) {
                    f32x4 a0 = Z4, a1 = Z4;
#pragma unroll
                    for (int s = 0; s < 4; ++s) {
                        const v4u bo = v4u{tr[nt][2 * s].x, tr[nt][2 * s].y, tr[nt][2 * s + 1].x, tr[nt][2 * s + 1].y};
                        a0 = mfma(u0[s], bo, a0); a1 = mfma(u1[s], bo, a1);
                    }
                    *(LAS v4u*)(L + w * 16384 + (ks * 4 + nt) * 1024 + lane * 16) =
                        v4u{pack2(a0.x, a0.y), pack2(a0.z, a0.w), pack2(a1.x, a1.y), pack2(a1.z, a1.w)};
                }
                FENCE();
            }
            __syncthreads();
        }
        STAMP(5 + 20 * layer);
        {
            IDS();
#pragma unroll
            for (int ks = 0; ks < 2; ++ks)
#pragma unroll
                for (int nt = 0; nt < 4; ++nt) wvo[ks][nt] = *(LAS v4u*)(L + w * 16384 + (ks * 4 + nt) * 1024 + lane * 16);
            if constexpr (layer == 1) nap(D_E2);
            const int b = w >> 2, chunk = lane & 7, sp0 = (tid >> 3) & 31;
            const int ib = WS_E2 + (h1 * 8 + j1) * 16384 + (b * 64 + sp0) * 128 + chunk * 16;
            const int sb = WS_ST + ((b * 32 + h1) * 64 + lane) * 8;
            v4u d0, d1; v2u ls;
            int spins = 0;
            for (;;) {
                d0 = ld16(c, ib); d1 = ld16(c, ib + 32 * 128); ls = ld8(c, sb);
                if (__all(okbit<T>(andor<T>(andor<T>(v4<T>(d0), v4<T>(d1)), andor<T>(ls.x, ls.y))))) break;
                if (give_up(c, spins)) break;
            }
            STAMP(6 + 20 * layer);
            LAS unsigned char* im = L + L_A + (b * 64 + sp0) * 128 + ((chunk ^ swz8(sp0)) * 16);
            *(LAS v4u*)im = d0; *(LAS v4u*)(im + 32 * 128) = d1;
            const float Mi = u2f(ls.x);
            const float e = __builtin_amdgcn_exp2f(Mi - wave_max(Mi));
            const float wn = e * __builtin_amdgcn_rcpf(wave_sum(e * u2f(ls.y)));
            v4u wf[2];
            mk_frags<false>(wn, 0.f, L + L_XS + w * 512, lane, wf);
            __syncthreads();
            {
                const int ct = w & 3, q = n >> 2, pp = n & 3;
                v2u t0, t1, t2, t3;
                const int r0 = 8 * g + q;
                const unsigned a0 = lds0 + L_A + (b * 64 + r0) * 128 + (((2 * ct + (pp >> 1)) ^ swz8(r0)) * 16) + 8 * (pp & 1);
                asm volatile("ds_read_b64_tr_b16 %0, %4\n\tds_read_b64_tr_b16 %1, %4 offset:512\n\tds_read_b64_tr_b16 %2, %4 offset:4096\n\tds_read_b64_tr_b16 %3, %4 offset:4608\n\ts_waitcnt lgkmcnt(0)"
                             : "=&v"(t0), "=&v"(t1), "=&v"(t2), "=&v"(t3) : "v"(a0) : "memory");
                f32x4 o = mfma(wf[0], v4u{t0.x, t0.y, t1.x, t1.y}, Z4);
                o = mfma(wf[1], v4u{t2.x, t2.y, t3.x, t3.y}, o);
                const float v = o.x + o.z;
                const float vn = dpp<0xB1>(v);
                const unsigned hi = pack2(v, vn);
                const unsigned lo = pack2(v - bflo(hi), vn - bfhi(hi));
                LAS unsigned* x2 = (LAS unsigned*)(L + L_X2) + b * 32 + 8 * ct + (n >> 1);
                if (lane < 16 && !(lane & 1)) { x2[0] = hi; x2[64] = lo; }
            }
            __syncthreads();
            v4u of[2];
#pragma unroll
            for (int ks = 0; ks < 2; ++ks) { v4u v = {0u, 0u, 0u, 0u}; if (n < 4) v = *(LAS v4u*)(L + L_X2 + n * 128 + 64 * ks + 16 * g); of[ks] = v; }
#pragma unroll
            for (int nt = 0; nt < 4; ++nt) {
                f32x4 a2 = mfma(of[0], wvo[0][nt], Z4);
                a2 = mfma(of[1], wvo[1][nt], a2);
                if (g == 0) st8(c, WS_E3 + (4 * w + nt) * 32768 + cu * 128 + n * 8, v2u{tagf<T>(a2.x + a2.z), tagf<T>(a2.y + a2.w)});
            }
            STAMP(7 + 20 * layer);
            if constexpr (layer == 0) {
                if (idx >= 16 && tid < 112) {
                    const unsigned wv = *(const GAS unsigned*)(p.w2 + (size_t)(112 * (idx - 16) + tid) * MD + 64 * xcd);
                    asm volatile("" ::"v"(wv));
                }
            }
        }
        auto ident = [&](int n, int g, int hi) {
            unsigned a[4];
#pragma unroll
            for (int q = 0; q < 4; ++q) { const int k0 = 8 * g + 2 * q - 16 * hi; a[q] = (k0 == n ? 0x3f80u : 0u) | (k0 + 1 == n ? 0x3f800000u : 0u); }
            return v4u{a[0], a[1], a[2], a[3]};
        };
        if (isRed) {
            IDS();
            nap(D_E3);
            const int ib = WS_E3 + red * 32768 + tid * 16;
            v4u d[4];
            int spins = 0;
            for (;;) {
#pragma unroll
                for (int k = 0; k < 4; ++k) d[k] = ld16(c, ib + k * 8192);
                if (__all(okbit<T>(andor<T>(andor<T>(v4<T>(d[0]), v4<T>(d[1])), andor<T>(v4<T>(d[2]), v4<T>(d[3])))))) break;
                if (give_up(c, spins)) break;
            }
            STAMP(8 + 20 * layer);
            float a[4];
            a[0] = (u2f(d[0].x) + u2f(d[1].x)) + (u2f(d[2].x) + u2f(d[3].x));
            a[1] = (u2f(d[0].y) + u2f(d[1].y)) + (u2f(d[2].y) + u2f(d[3].y));
            a[2] = (u2f(d[0].z) + u2f(d[1].z)) + (u2f(d[2].z) + u2f(d[3].z));
            a[3] = (u2f(d[0].w) + u2f(d[1].w)) + (u2f(d[2].w) + u2f(d[3].w));
#pragma unroll
            for (int i = 0; i < 4; ++i) { a[i] += dpp<0x128>(a[i]); a[i] = x32_sum(x16_sum(a[i])); }
            LAS float* rd = (LAS float*)(L + L_RED);
            if (lane < 8) *(LAS f32x4*)(rd + w * 32 + lane * 4) = f32x4{a[0], a[1], a[2], a[3]};
            __syncthreads();
            if (tid < 32) {
                float v = 0.f;
#pragma unroll
                for (int i = 0; i < 8; ++i) v += rd[i * 32 + tid];
                xres += v;
                st4(c, WS_E4 + red * 128 + tid * 4, tagf<T>(xres));
            }
            STAMP(9 + 20 * layer);
            if constexpr (layer == 0) {
                const u16* pw = p.w2 + (size_t)(224 * w + n) * MD + 32 * (red >> 1) + 8 * g;
                const v4u Isel = ident(n, g, red & 1);
                v4u raw[14];
#pragma unroll
                for (int ft = 0; ft < 14; ++ft) raw[ft] = ldnt(pw + (size_t)ft * 16 * MD);
                FENCE();
#pragma unroll
                for (int ks = 0; ks < 7; ++ks) {
                    const f32x4 a0 = mfma(raw[2 * ks], Isel, Z4), a1 = mfma(raw[2 * ks + 1], Isel, Z4);
                    glf[ks >> 2][ks & 3] = v4u{pack2(a0.x, a0.y), pack2(a0.z, a0.w), pack2(a1.x, a1.y), pack2(a1.z, a1.w)};
                    pin(glf[ks >> 2][ks & 3]);
                }
            }
        }
        if (isGU) {
            IDS();
            if constexpr (layer == 0) {
                const v4u Ilo = ident(n, g, 0), Ihi = ident(n, g, 1);
                v4u raw[2][4];
#pragma unroll
                for (int mt = 0; mt < 4; ++mt) {
                    raw[0][mt] = ldnt(p.w1 + (size_t)(64 * w + 16 * mt + n) * FF + 32 * gu + 8 * g);
                    raw[1][mt] = ldnt(p.w3 + (size_t)(64 * w + 16 * mt + n) * FF + 32 * gu + 8 * g);
                }
                FENCE();
#pragma unroll
                for (int mat = 0; mat < 2; ++mat)
#pragma unroll
                    for (int ks = 0; ks < 2; ++ks) {
                        const f32x4 l0 = mfma(raw[mat][2 * ks], Ilo, Z4), h0 = mfma(raw[mat][2 * ks], Ihi, Z4);
                        const f32x4 l1 = mfma(raw[mat][2 * ks + 1], Ilo, Z4), hh1 = mfma(raw[mat][2 * ks + 1], Ihi, Z4);
                        glf[ks][mat * 2] = v4u{pack2(l0.x, l0.y), pack2(l0.z, l0.w), pack2(l1.x, l1.y), pack2(l1.z, l1.w)};
                        glf[ks][mat * 2 + 1] = v4u{pack2(h0.x, h0.y), pack2(h0.z, h0.w), pack2(hh1.x, hh1.y), pack2(hh1.z, hh1.w)};
                        pin(glf[ks][mat * 2]); pin(glf[ks][mat * 2 + 1]);
                    }
            } else nap(D_E4);
            float x0, x1;
            int spins = 0;
            for (;;) {
                const v2u v = ld8(c, WS_E4 + tid * 8), s = ld8(c, WS_E4);
                x0 = u2f(v.x) - u2f(s.x); x1 = u2f(v.y) - u2f(s.y);
                if (__all(okbit<T>(andor<T>(andor<T>(v.x, v.y), andor<T>(s.x, s.y))))) break;
                if (give_up(c, spins)) break;
            }
            STAMP(10 + 20 * layer);
            ln_gemv_partials(x0, x1, glf, L, w, lane);
            __syncthreads();
            if (tid < 32) {
                float o[4];
                ln_gemv_final(L, tid, o);
                float u[4];
#pragma unroll
                for (int i = 0; i < 4; ++i) { auto r = __builtin_amdgcn_permlane16_swap(f2u(o[i]), f2u(o[i]), false, false); u[i] = u2f(r[1]); }
                if (tid < 16) {
                    float hv[4];
#pragma unroll
                    for (int i = 0; i < 4; ++i) hv[i] = o[i] * __builtin_amdgcn_rcpf(1.0f + __builtin_amdgcn_exp2f(-1.4426950408889634f * o[i])) * u[i];
                    st16(c, WS_E5 + gu * 256 + tid * 16, v4u{tagf<T>(hv[0]), tagf<T>(hv[1]), tagf<T>(hv[2]), tagf<T>(hv[3])});
                }
            }
            STAMP(11 + 20 * layer);
        }
        if (isRed) {
            IDS();
            if constexpr (layer == 1) nap(D_E5);
            const int ib = WS_E5 + (112 * w + lane) * 16;
            const int ib1 = ib + (lane < 48 ? 1024 : 0);
            v4u d0, d1;
            int spins = 0;
            for (;;) {
                d0 = ld16(c, ib); d1 = ld16(c, ib1);
                if (__all(okbit<T>(andor<T>(v4<T>(d0), v4<T>(d1))))) break;
                if (give_up(c, spins)) break;
            }
            STAMP(12 + 20 * layer);
            LAS unsigned char* hs = L + L_A + w * 1856;
#pragma unroll
            for (int k = 0; k < 2; ++k) {
                const v4u d = k ? d1 : d0;
                const int f = 2 * (lane + 64 * k);
                const int pos = (f & ~31) + 8 * ((f >> 2) & 3) + 4 * ((f >> 4) & 1) + (f & 3);
                const unsigned h0 = pack2(u2f(d.x), u2f(d.z)), hb1 = pack2(u2f(d.y), u2f(d.w));
                const unsigned l0 = pack2(u2f(d.x) - bflo(h0), u2f(d.z) - bfhi(h0)), lb1 = pack2(u2f(d.y) - bflo(hb1), u2f(d.w) - bfhi(hb1));
                if (k == 0 || lane < 48) {
                    *(LAS unsigned*)(hs + pos * 2) = h0; *(LAS unsigned*)(hs + 464 + pos * 2) = hb1;
                    *(LAS unsigned*)(hs + 928 + pos * 2) = l0; *(LAS unsigned*)(hs + 1392 + pos * 2) = lb1;
                }
            }
            f32x4 a = Z4, a2 = Z4;
#pragma unroll
            for (int ks = 0; ks < 7; ++ks) {
                v4u v = {0u, 0u, 0u, 0u};
                if (n < 4) v = *(LAS v4u*)(hs + n * 464 + 64 * ks + 16 * g);
                if (ks & 1) a2 = mfma(v, glf[ks >> 2][ks & 3], a2); else a = mfma(v, glf[ks >> 2][ks & 3], a);
            }
            a += a2;
            LAS float* rd = (LAS float*)(L + L_RED);
            if (g == 0) *(LAS f32x2*)(rd + w * 32 + n * 2) = f32x2{a.x + a.z, a.y + a.w};
            __syncthreads();
            if (tid < 32) {
                float v = 0.f;
#pragma unroll
                for (int i = 0; i < 8; ++i) v += rd[i * 32 + tid];
                xres += v;
                if constexpr (layer == 0) st4(c, WS_E6 + red * 128 + tid * 4, tagf<1u>(xres));
                else p.out[(tid & 1) * MD + 16 * red + (tid >> 1)] = (u16)pack2(xres, 0.f);
            }
            STAMP(13 + 20 * layer);
        }
    };
    layer_body(std::integral_constant<int, 0>{});
    layer_body(std::integral_constant<int, 1>{});
    asm volatile("mk_code_end:");
}
__global__ void zk(v4u* p, int n) { for (int i = blockIdx.x * 256 + threadIdx.x; i < n; i += 32 * 256) p[i] = v4u{0u, 0u, 0u, 0u}; }
}

extern "C" void kernel_launch(void* const* d_in, const int* in_sizes, int n_in,
                              void* d_out, int out_size, void* d_ws, size_t ws_size,
                              hipStream_t stream) {
    mk::Params p;
    p.x = (const u16*)d_in[0]; p.ckv = (const u16*)d_in[1]; p.wq = (const u16*)d_in[2]; p.wuk = (const u16*)d_in[3];
    p.wuv = (const u16*)d_in[4]; p.wo = (const u16*)d_in[5]; p.w1 = (const u16*)d_in[7]; p.w3 = (const u16*)d_in[8];
    p.w2 = (const u16*)d_in[9]; p.out = (u16*)d_out; p.ws = (unsigned char*)d_ws;
    hipLaunchKernelGGL(mk::zk, dim3(32), dim3(256), 0, stream, (v4u*)d_ws, mk::WS_ZERO_BYTES / 16);
    hipLaunchKernelGGL(mk::mega, dim3(mk::NWG), dim3(mk::NT), 0, stream, p);
}
```

```cpp
#include <hip/hip_runtime.h>
#include <hip/hip_bf16.h>
#include <type_traits>
#include <cstdio>

typedef unsigned short u16;
typedef unsigned v4u __attribute__((ext_vector_type(4)));
typedef unsigned v2u __attribute__((ext_vector_type(2)));
typedef float f32x4 __attribute__((ext_vector_type(4)));
typedef float f32x2 __attribute__((ext_vector_type(2)));
typedef short bf16x8 __attribute__((ext_vector_type(8)));
typedef __bf16 bf16x2 __attribute__((ext_vector_type(2)));
#define GAS __attribute__((address_space(1)))
#define LAS __attribute__((address_space(3)))
#define DI __device__ __forceinline__
#define RLX_AGENT __ATOMIC_RELAXED, __HIP_MEMORY_SCOPE_AGENT

#define STAMPS 1
#define WARMPOL "nt"
#define CKV_V 3
#define PROF 0

namespace mk {
constexpr int NWG = 256, NT = 512;
constexpr int SK = 8192, DC = 512, MD = 512, HD = 4096, FF = 1792;
constexpr int WS_CTRL = 0;
constexpr int WS_E1 = 256;
constexpr int WS_ST = WS_E1 + 65536;
constexpr int WS_E4 = WS_ST + 32768;
constexpr int WS_E5 = WS_E4 + 4096;
constexpr int WS_E6 = WS_E5 + 14336;
constexpr int WS_ZERO_BYTES = WS_E6 + 4096;
constexpr int WS_E2 = 131072;
constexpr int WS_E3 = WS_E2 + 256 * 16384;
constexpr int WS_STAMP = WS_E3 + 32 * 32768;
constexpr int WS_BYTES = WS_STAMP + 256 * 64 * 8;
static_assert(WS_ZERO_BYTES <= WS_E2 && (WS_ZERO_BYTES % 16) == 0, "ws map");
constexpr int L_A = 131072;
constexpr int L_TQ = L_A + 16384;
constexpr int L_RED = L_A + 17408;
constexpr int L_P = L_RED;
constexpr int L_SMAX = L_RED + 6400;
constexpr int L_XS = L_SMAX + 512;
constexpr int L_X2 = L_XS + 4096;
constexpr int L_BYTES = 163840;
static_assert(L_X2 + 512 <= L_BYTES, "LDS");

struct Params { const u16 *x, *ckv, *wq, *wuk, *wuv, *wo, *w1, *w3, *w2; u16* out; unsigned char* ws; };

DI float u2f(unsigned u) { return __builtin_bit_cast(float, u); }
DI unsigned f2u(float f) { return __builtin_bit_cast(unsigned, f); }
DI f32x4 mfma(v4u a, v4u b, f32x4 c) {
    return __builtin_amdgcn_mfma_f32_16x16x32_bf16(__builtin_bit_cast(bf16x8, a), __builtin_bit_cast(bf16x8, b), c, 0, 0, 0);
}
DI unsigned pack2(float a, float b) { bf16x2 v = {(__bf16)a, (__bf16)b}; return __builtin_bit_cast(unsigned, v); }
DI float bflo(unsigned u) { return u2f(u << 16); }
DI float bfhi(unsigned u) { return u2f(u & 0xffff0000u); }
template <unsigned T> DI unsigned pack2_tag(float a, float b) {
    const unsigned u = f2u(a);
    const unsigned lo = T ? ((u >> 16) | 1u) : (((u + 0x10000u) >> 17) << 1);
    return __builtin_amdgcn_perm(pack2(b, b), lo, 0x07060100u);
}
template <unsigned T> DI unsigned tagf(float a) { return T ? (f2u(a) | 1u) : (f2u(a) & ~1u); }
DI v4u ldnt(const u16* p) { return __builtin_nontemporal_load((const GAS v4u*)p); }
DI void glds16(const void* gsrc, unsigned lds_dst) {
    unsigned keep;
    asm volatile("s_mov_b32 %0, m0\n\ts_mov_b32 m0, %2\n\ts_nop 0\n\tglobal_load_lds_dwordx4 %1, off nt\n\ts_mov_b32 m0, %0" : "=&s"(keep) : "v"(gsrc), "s"(lds_dst) : "memory");
}
#define LDNT_ASM(dst, ptr) asm volatile("global_load_dwordx4 %0, %1, off nt" : "=v"(dst) : "v"(ptr) : "memory")
DI int swz16(int r) { return ((r >> 3) & 1) | ((r & 3) << 1) | ((((r >> 2) ^ (r >> 3)) & 1) << 3); }
DI int swz8(int r) { return (r & 2) | ((r >> 1) & 4); }
#define TR_ISSUE(dst, addr, OFF) asm volatile("ds_read_b64_tr_b16 %0, %1 offset:" #OFF : "=v"(dst) : "v"(addr) : "memory")

template <int CTRL> DI unsigned dppu(unsigned v) { return (unsigned)__builtin_amdgcn_update_dpp(0, (int)v, CTRL, 0xf, 0xf, true); }
template <int CTRL> DI float dpp(float v) { return u2f(dppu<CTRL>(f2u(v))); }
DI float row_sum16(float v) { v += dpp<0xB1>(v); v += dpp<0x4E>(v); v += dpp<0x141>(v); v += dpp<0x140>(v); return v; }
DI float row_max16(float v) { v = fmaxf(v, dpp<0xB1>(v)); v = fmaxf(v, dpp<0x4E>(v)); v = fmaxf(v, dpp<0x141>(v)); v = fmaxf(v, dpp<0x140>(v)); return v; }
DI float x16_sum(float v) { auto r = __builtin_amdgcn_permlane16_swap(f2u(v), f2u(v), false, false); return u2f(r[0]) + u2f(r[1]); }
DI float x32_sum(float v) { auto r = __builtin_amdgcn_permlane32_swap(f2u(v), f2u(v), false, false); return u2f(r[0]) + u2f(r[1]); }
DI float x16_max(float v) { auto r = __builtin_amdgcn_permlane16_swap(f2u(v), f2u(v), false, false); return fmaxf(u2f(r[0]), u2f(r[1])); }
DI float x32_max(float v) { auto r = __builtin_amdgcn_permlane32_swap(f2u(v), f2u(v), false, false); return fmaxf(u2f(r[0]), u2f(r[1])); }
DI float wave_sum(float v) { return x32_sum(x16_sum(row_sum16(v))); }
DI float wave_max(float v) { return x32_max(x16_max(row_max16(v))); }
DI void pin(v4u& v) { asm volatile("" : "+v"(v)); }
DI void nap(int n) {
#pragma unroll 1
    for (int i = 0; i < n; ++i) __builtin_amdgcn_s_sleep(1);
}

struct Ctx {
    __amdgpu_buffer_rsrc_t rs;
    GAS unsigned* abortw;
    unsigned long long t0;
    bool dead;
};
DI v4u ld16(const Ctx& c, int off) { return __builtin_amdgcn_raw_buffer_load_b128(c.rs, off, 0, 16); }
DI v2u ld8(const Ctx& c, int off) { return __builtin_amdgcn_raw_buffer_load_b64(c.rs, off, 0, 16); }
DI void st16(const Ctx& c, int off, v4u v) { __builtin_amdgcn_raw_buffer_store_b128(v, c.rs, off, 0, 16); }
DI void st8(const Ctx& c, int off, v2u v) { __builtin_amdgcn_raw_buffer_store_b64(v, c.rs, off, 0, 16); }
DI void st4(const Ctx& c, int off, unsigned v) { __builtin_amdgcn_raw_buffer_store_b32(v, c.rs, off, 0, 16); }
DI bool give_up(Ctx& c, int& spins) {
    if (c.dead) return true;
    if ((++spins & 63) == 0) {
        const bool late = (long long)(__builtin_amdgcn_s_memrealtime() - c.t0) > 5000000ll;
        if (late || __hip_atomic_load(c.abortw, RLX_AGENT) != 0u) { __hip_atomic_store(c.abortw, 1u, RLX_AGENT); c.dead = true; return true; }
    }
    return false;
}
template <unsigned T> DI unsigned andor(unsigned a, unsigned b) { return T ? (a & b) : (a | b); }
template <unsigned T> DI unsigned v4(v4u v) { return andor<T>(andor<T>(v.x, v.y), andor<T>(v.z, v.w)); }
template <unsigned T> DI bool okbit(unsigned m) { return (m & 1u) == T; }

template <bool PERM> DI void mk_frags(float v0, float v1, LAS unsigned char* xs, int lane, v4u (&fr)[2]) {
    const int n = lane & 15, g = lane >> 4;
    const unsigned hi = pack2(v0, v1);
    const unsigned lo = pack2(v0 - bflo(hi), v1 - bfhi(hi));
    const bool odd = lane & 1;
    const unsigned recv = dppu<0xB1>(odd ? hi : lo);
    const unsigned a = odd ? recv : hi, b = odd ? lo : recv;
    const unsigned w0 = __builtin_amdgcn_perm(b, a, 0x05040100u), w1 = __builtin_amdgcn_perm(b, a, 0x07060302u);
    const int pos2 = PERM ? (16 * (lane >> 5) + 4 * ((lane >> 2) & 3) + 2 * ((lane >> 4) & 1) + ((lane >> 1) & 1)) : (lane >> 1);
    LAS unsigned* x32 = (LAS unsigned*)xs + (odd ? 64 : 0) + pos2;
    x32[0] = w0; x32[32] = w1;
    const unsigned cst = n == 4 ? 0x3f803f80u : 0u;
#pragma unroll
    for (int ks = 0; ks < 2; ++ks) {
        v4u v = {cst, cst, cst, cst};
        if (n < 4) v = *(LAS v4u*)(xs + n * 128 + 64 * ks + 16 * g);
        fr[ks] = v;
    }
}

#define IDS() int tid = threadIdx.x; asm volatile("" : "+v"(tid)); const int lane = tid & 63, n = lane & 15, g = lane >> 4; (void)n; (void)g; (void)lane
#define FENCE() __builtin_amdgcn_sched_barrier(0)
#define STAMP(i) do { stv[i] = (unsigned)__builtin_amdgcn_s_memtime(); } while (0)

#define D_E1A 0
#define D_E1 24
#define D_E2 40
#define D_E3 20
#define D_E4 70
#define D_E5 50
#define D_E6 130
#define D_E6R 14
#define D_E6G 50

DI void ln_gemv_partials(float x0, float x1, const v4u (&bf)[2][4], LAS unsigned char* L, int w, int lane) {
    const int n = lane & 15, g = lane >> 4;
    v4u xf[2];
    mk_frags<true>(x0, x1, L + L_XS + w * 512, lane, xf);
    LAS unsigned char* rq = L + L_RED + w * 512 + n * 8;
    LAS unsigned char* rc = L + L_RED + 4096 + w * 256 + n * 4;
#pragma unroll
    for (int ct = 0; ct < 4; ++ct) {
        f32x4 a = mfma(xf[0], bf[0][ct], f32x4{0.f, 0.f, 0.f, 0.f});
        a = mfma(xf[1], bf[1][ct], a);
        if (g == 0) *(LAS f32x2*)(rq + ct * 128) = f32x2{a.x + a.z, a.y + a.w};
        if (g == 1) *(LAS float*)(rc + ct * 64) = a.x;
    }
    f32x4 gm = mfma(xf[0], xf[0], f32x4{0.f, 0.f, 0.f, 0.f});
    gm = mfma(xf[1], xf[1], gm);
    LAS float* rs = (LAS float*)(L + L_RED + 6144 + w * 16);
    if (lane == 4) *(LAS f32x2*)rs = f32x2{gm.x + gm.z, gm.y + gm.w};
    if (lane < 2) rs[2 + lane] = lane == 0 ? gm.x + 2.f * gm.z : gm.y + 2.f * gm.w;
}
DI void ln_gemv_final(LAS unsigned char* L, int t, float (&o)[4]) {
    f32x4 q = {0.f, 0.f, 0.f, 0.f}, st = {0.f, 0.f, 0.f, 0.f};
    f32x2 cs = {0.f, 0.f};
#pragma unroll
    for (int i = 0; i < 8; ++i) {
        q += *(LAS f32x4*)(L + L_RED + i * 512 + t * 16);
        cs += *(LAS f32x2*)(L + L_RED + 4096 + i * 256 + t * 8);
        st += *(LAS f32x4*)(L + L_RED + 6144 + i * 16);
    }
    const float m0 = st.x * (1.f / 512), m1 = st.y * (1.f / 512);
    const float r0 = __builtin_amdgcn_rsqf(fmaxf(st.z * (1.f / 512) - m0 * m0, 0.f) + 1e-5f);
    const float r1 = __builtin_amdgcn_rsqf(fmaxf(st.w * (1.f / 512) - m1 * m1, 0.f) + 1e-5f);
    o[0] = (q.x - m0 * cs.x) * r0; o[1] = (q.y - m1 * cs.x) * r1;
    o[2] = (q.z - m0 * cs.y) * r0; o[3] = (q.w - m1 * cs.y) * r1;
}

__global__ void __launch_bounds__(NT, 2) mega(Params p) {
    __shared__ __attribute__((aligned(16))) unsigned char smem[L_BYTES];
    LAS unsigned char* L = (LAS unsigned char*)smem;
    const int tid = threadIdx.x, lane = tid & 63, w = __builtin_amdgcn_readfirstlane(tid >> 6), n = lane & 15, g = lane >> 4;
    const int cu = blockIdx.x, xcd = cu & 7, idx = cu >> 3;
    const int h1 = xcd * 4 + (idx >> 3), j1 = idx & 7;
    const int gs = xcd * 16 + ((idx & 7) | ((idx >> 4) << 3)), bb = gs >> 6, split = gs & 63, hg = 1 - ((idx >> 3) & 1);
    const bool isRed = idx < 4, isGU = idx >= 8 && idx < 15;
    const int red = xcd * 4 + idx, gu = xcd * 7 + (idx - 8);
    unsigned cwarm = 0;
    {
        unsigned long long pc; unsigned len;
        asm volatile("mk_code_begin:\n\ts_getpc_b64 %0\n\ts_mov_b32 %1, mk_code_end-mk_code_begin" : "=s"(pc), "=s"(len));
        const unsigned long long ca = pc + (((unsigned)tid * 128u < len) ? (unsigned)tid * 128u : 0u);
        asm volatile("global_load_dword %0, %1, off" : "=v"(cwarm) : "v"(ca) : "memory");
    }
    Ctx c;
    c.rs = __builtin_amdgcn_make_buffer_rsrc((void*)p.ws, 0, WS_BYTES, 0x00020000);
    c.abortw = (GAS unsigned*)(p.ws + WS_CTRL);
    c.t0 = __builtin_amdgcn_s_memrealtime();
    c.dead = false;
    unsigned stv[40];
#pragma unroll
    for (int i = 0; i < 40; ++i) stv[i] = 0;
    STAMP(0);
    const f32x4 Z4 = {0.f, 0.f, 0.f, 0.f};

    v4u ck[16];
    v4u ckT[4][4];
    v4u glf[2][4];
    const u16* ckv0 = p.ckv + ((size_t)bb * SK + (size_t)split * 128) * DC;
    unsigned xr0, xr1, xr2, xs0, xs1;
    {
        const u16* px = p.x + tid;
        const u16* pr = p.x + ((tid & 1) * MD + 16 * (red & 31) + ((tid >> 1) & 15));
        asm volatile("global_load_ushort %0, %5, off\n\tglobal_load_ushort %1, %5, off offset:1024\n\tglobal_load_ushort %2, %6, off\n\t"
                     "global_load_ushort %3, %7, off\n\tglobal_load_ushort %4, %7, off offset:1024"
                     : "=&v"(xr0), "=&v"(xr1), "=&v"(xr2), "=&v"(xs0), "=&v"(xs1) : "v"(px), "v"(pr), "v"(p.x) : "memory");
    }

    v4u wqk[2][4];
    const unsigned lds0 = (unsigned)(size_t)smem;
    auto ckv_rows = [&]() {
#pragma unroll 4
        for (int i = 0; i < 16; ++i) glds16(ckv0 + (size_t)(16 * w + i) * DC + ((lane ^ swz16(i)) * 8), lds0 + (16 * w + i) * 1024);
    };
    {
        v4u aq[4][4];
#pragma unroll
        for (int i = 0; i < 2; ++i) {
            const int r = 8 * w + 4 * i + g;
            glds16(p.wuk + (size_t)(64 * j1 + r) * HD + h1 * 128 + ((n ^ swz16(r & 15)) * 8), lds0 + L_A + (8 * w + 4 * i) * 256);
        }
        {
            const u16* pq = p.wq + (size_t)(64 * w + g) * HD + h1 * 128 + 8 * n;
#pragma unroll
            for (int mt = 0; mt < 4; ++mt)
#pragma unroll
                for (int i = 0; i < 4; ++i) { const u16* pp = pq + (size_t)(16 * mt + 4 * i) * HD; LDNT_ASM(aq[mt][i], pp); }
        }
        asm volatile("s_waitcnt vmcnt(16)" : "+v"(xr0), "+v"(xr1), "+v"(xr2), "+v"(xs0), "+v"(xs1), "+v"(cwarm) :: "memory");
        STAMP(14);
        __syncthreads();
        STAMP(15);
        const int fs = swz16(n);
        v4u bu[4][4];
#pragma unroll
        for (int ct = 0; ct < 4; ++ct)
#pragma unroll
            for (int s = 0; s < 4; ++s) bu[ct][s] = *(LAS v4u*)(L + L_A + (16 * ct + n) * 256 + (((4 * s + g) ^ fs) * 16));
#pragma unroll
        for (int ct = 0; ct < 4; ++ct)
#pragma unroll
            for (int s = 0; s < 4; ++s) pin(bu[ct][s]);
        __syncthreads();
        STAMP(16);
        if (w < 4) ckv_rows();
        else {
            const v4u z = {0u, 0u, 0u, 0u};
            const int t = tid - 256;
            const int ib = WS_E2 + (h1 * 8 + j1) * 16384 + t * 16;
#pragma unroll
            for (int i = 0; i < 4; ++i) st16(c, ib + i * 4096, z);
            st16(c, WS_E3 + cu * 4096 + t * 16, z);
        }
        STAMP(17);
#define WAITQ(NA, NB, mt) asm volatile("s_cmp_lt_u32 %4, 4\n\ts_cbranch_scc1 1f\n\ts_waitcnt vmcnt(" #NB ")\n1:\n\ts_waitcnt vmcnt(" #NA ")" \
            : "+v"(aq[mt][0]), "+v"(aq[mt][1]), "+v"(aq[mt][2]), "+v"(aq[mt][3]) : "s"(w) : "memory", "scc")
        LAS unsigned char* tq = L + L_A + w * 4096;
#pragma unroll
        for (int mt = 0; mt < 4; ++mt) {
            if (mt == 0) WAITQ(28, 17, 0);
            else if (mt == 1) WAITQ(24, 13, 1);
            else if (mt == 2) WAITQ(20, 9, 2);
            else { WAITQ(16, 0, 3); STAMP(18); }
#pragma unroll
            for (int i = 0; i < 4; ++i) { const int r = 4 * i + g; *(LAS v4u*)(tq + r * 256 + ((n ^ swz16(r)) * 16)) = aq[mt][i]; }
            v4u af[4];
#pragma unroll
            for (int s = 0; s < 4; ++s) af[s] = *(LAS v4u*)(tq + n * 256 + (((4 * s + g) ^ fs) * 16));
#pragma unroll
            for (int ct = 0; ct < 4; ++ct) {
                f32x4 a0 = Z4;
#pragma unroll
                for (int s = 0; s < 4; ++s) a0 = mfma(af[s], bu[ct][s], a0);
                if (mt & 1) { wqk[mt >> 1][ct].z = pack2(a0.x, a0.y); wqk[mt >> 1][ct].w = pack2(a0.z, a0.w); }
                else { wqk[mt >> 1][ct].x = pack2(a0.x, a0.y); wqk[mt >> 1][ct].y = pack2(a0.z, a0.w); }
            }
        }
        if (w >= 4) ckv_rows();
#pragma unroll
        for (int ks = 0; ks < 2; ++ks)
#pragma unroll
            for (int ct = 0; ct < 4; ++ct) pin(wqk[ks][ct]);
    }
    __syncthreads();
    STAMP(1);
    float xres = bflo(xr2);

    auto layer_body = [&](auto LC) {
        constexpr int layer = decltype(LC)::value;
        constexpr unsigned T = layer == 0 ? 1u : 0u;
        {
            IDS();
            float x0, x1;
            if constexpr (layer == 0) { x0 = bflo(xr0) - bflo(xs0); x1 = bflo(xr1) - bflo(xs1); }
            else {
#pragma unroll
                for (int ks = 0; ks < 2; ++ks)
#pragma unroll
                    for (int ct = 0; ct < 4; ++ct) wqk[ks][ct] = *(LAS v4u*)(L + w * 16384 + 8192 + (ks * 4 + ct) * 1024 + lane * 16);
                nap(isRed ? D_E6R : (isGU ? D_E6G : D_E6));
                int spins = 0;
                for (;;) {
                    const v2u v = ld8(c, WS_E6 + tid * 8), s = ld8(c, WS_E6);
                    x0 = u2f(v.x) - u2f(s.x); x1 = u2f(v.y) - u2f(s.y);
                    if (__all(okbit<1u>(v.x & v.y & s.x & s.y))) break;
                    if (give_up(c, spins)) break;
                }
                STAMP(20);
            }
            ln_gemv_partials(x0, x1, wqk, L, w, lane);
            __syncthreads();
            if (tid < 32) {
                float o[4];
                ln_gemv_final(L, tid, o);
                const float SC = 0.08838834764831845f * 1.4426950408889634f;
                const int dst = WS_E1 + ((h1 & 1) * 16 + (h1 >> 1)) * 1024 + (64 * j1 + 2 * tid) * 2;
                st4(c, dst, pack2_tag<T>(o[0] * SC, o[2] * SC));
                st4(c, dst + 32768, pack2_tag<T>(o[1] * SC, o[3] * SC));
            }
            STAMP(2 + 20 * layer);
        }
        v4u wuvr[2];
        {
            IDS();
            if constexpr (layer == 0) {
                {
                    const u16* pu = p.wuv + (size_t)(64 * j1 + (tid >> 4)) * HD + h1 * 128 + (tid & 15) * 8;
                    const u16* pu2 = pu + (size_t)32 * HD;
                    LDNT_ASM(wuvr[0], pu); LDNT_ASM(wuvr[1], pu2);
                }
                asm volatile("s_waitcnt vmcnt(2)" ::: "memory");
                STAMP(19);
                __syncthreads();
                const int fs = swz16(n);
#pragma unroll
                for (int s = 0; s < 16; ++s) ck[s] = *(LAS v4u*)(L + (16 * w + n) * 1024 + (((4 * s + g) ^ fs) * 16));
                v2u tr[4][8];
                const int q = n >> 2, pp = n & 3;
#pragma unroll
                for (int t = 0; t < 2; ++t) {
                    const int r = 8 * g + 4 * t + q;
#pragma unroll
                    for (int ct = 0; ct < 4; ++ct) {
                        const unsigned ad = lds0 + r * 1024 + (((8 * w + 2 * ct + (pp >> 1)) ^ swz16(r & 15)) * 16) + 8 * (pp & 1);
                        TR_ISSUE(tr[ct][0 + t], ad, 0); TR_ISSUE(tr[ct][2 + t], ad, 32768);
                        const unsigned ad2 = ad + 65536;
                        TR_ISSUE(tr[ct][4 + t], ad2, 0); TR_ISSUE(tr[ct][6 + t], ad2, 32768);
                    }
                }
                asm volatile("s_waitcnt lgkmcnt(0)" : "+v"(tr[0][0]), "+v"(tr[0][1]), "+v"(tr[0][2]), "+v"(tr[0][3]), "+v"(tr[0][4]), "+v"(tr[0][5]), "+v"(tr[0][6]), "+v"(tr[0][7]),
                             "+v"(tr[1][0]), "+v"(tr[1][1]), "+v"(tr[1][2]), "+v"(tr[1][3]), "+v"(tr[1][4]), "+v"(tr[1][5]), "+v"(tr[1][6]), "+v"(tr[1][7]) :: "memory");
                asm volatile("" : "+v"(tr[2][0]), "+v"(tr[2][1]), "+v"(tr[2][2]), "+v"(tr[2][3]), "+v"(tr[2][4]), "+v"(tr[2][5]), "+v"(tr[2][6]), "+v"(tr[2][7]),
                             "+v"(tr[3][0]), "+v"(tr[3][1]), "+v"(tr[3][2]), "+v"(tr[3][3]), "+v"(tr[3][4]), "+v"(tr[3][5]), "+v"(tr[3][6]), "+v"(tr[3][7]) :: "memory");
#pragma unroll
                for (int ct = 0; ct < 4; ++ct)
#pragma unroll
                    for (int s = 0; s < 4; ++s) ckT[ct][s] = v4u{tr[ct][2 * s].x, tr[ct][2 * s].y, tr[ct][2 * s + 1].x, tr[ct][2 * s + 1].y};
#pragma unroll
                for (int s = 0; s < 16; ++s) pin(ck[s]);
                __syncthreads();
                STAMP(17);
                if (w >= 4) {
                    const int rr = lane >> 3;
#pragma unroll 4
                    for (int i = 0; i < 32; ++i) {
                        const int v = (i & 1) ? w : w - 4, d = 8 * (i >> 1) + rr;
                        glds16(p.wo + (size_t)(h1 * 128 + d) * MD + 64 * v + (((lane & 7) ^ swz8(d)) * 8), lds0 + v * 16384 + (i >> 1) * 1024);
                    }
                }
            }
            if (w < 4) {
                const int t = tid;
                v4u qg[4];
                const int e1 = WS_E1 + (bb * 2 + hg) * 16384 + t * 16;
                nap(layer == 0 ? D_E1A : D_E1);
                int spins = 0;
                for (;;) {
#pragma unroll
                    for (int k = 0; k < 4; ++k) qg[k] = ld16(c, e1 + k * 4096);
                    if (__all(okbit<T>(andor<T>(andor<T>(v4<T>(qg[0]), v4<T>(qg[1])), andor<T>(v4<T>(qg[2]), v4<T>(qg[3])))))) break;
                    if (give_up(c, spins)) break;
                }
#pragma unroll
                for (int k = 0; k < 4; ++k) { const int u = t + 256 * k; *(LAS v4u*)(L + L_A + (u >> 6) * 1040 + (u & 63) * 16) = qg[k]; }
            }
            __syncthreads();
            STAMP(3 + 20 * layer);
        }
        {
            IDS();
            f32x4 s0 = Z4, s1 = Z4;
#pragma unroll
            for (int s = 0; s < 16; s += 2) {
                const v4u q0 = *(LAS v4u*)(L + L_A + n * 1040 + 64 * s + 16 * g);
                const v4u q1 = *(LAS v4u*)(L + L_A + n * 1040 + 64 * (s + 1) + 16 * g);
                s0 = mfma(ck[s], q0, s0); s1 = mfma(ck[s + 1], q1, s1);
            }
            s0 += s1;
            float mx = fmaxf(fmaxf(s0.x, s0.y), fmaxf(s0.z, s0.w));
            mx = x32_max(x16_max(mx));
            LAS float* smax = (LAS float*)(L + L_SMAX);
            if (g == 0) smax[n * 8 + w] = mx;
            __syncthreads();
            if constexpr (layer == 0) {
                asm volatile("s_cmp_lt_u32 %2, 4\n\ts_cbranch_scc1 1f\n\ts_waitcnt vmcnt(32)\n\ts_branch 2f\n1:\n\ts_waitcnt vmcnt(0)\n2:" : "+v"(wuvr[0]), "+v"(wuvr[1]) : "s"(w) : "memory", "scc");
                const int r = tid >> 4;
                LAS unsigned char* dst = L + L_A + r * 256 + (((tid & 15) ^ swz16(r & 15)) * 16);
                *(LAS v4u*)dst = wuvr[0]; *(LAS v4u*)(dst + 32 * 256) = wuvr[1];
            }
            const f32x4 ma = *(LAS f32x4*)(smax + n * 8), mb = *(LAS f32x4*)(smax + n * 8 + 4);
            const float M = fmaxf(fmaxf(fmaxf(ma.x, ma.y), fmaxf(ma.z, ma.w)), fmaxf(fmaxf(mb.x, mb.y), fmaxf(mb.z, mb.w)));
            const unsigned p01 = pack2(__builtin_amdgcn_exp2f(s0.x - M), __builtin_amdgcn_exp2f(s0.y - M));
            const unsigned p23 = pack2(__builtin_amdgcn_exp2f(s0.z - M), __builtin_amdgcn_exp2f(s0.w - M));
            *(LAS v2u*)(L + L_P + n * 272 + (16 * w + 4 * g) * 2) = v2u{p01, p23};
            __syncthreads();
            v4u pf[4];
#pragma unroll
            for (int s = 0; s < 4; ++s) pf[s] = *(LAS v4u*)(L + L_P + n * 272 + 64 * s + 16 * g);
            const int h = 2 * n + hg;
            const int dst = WS_E2 + (h * 8 + w) * 16384 + (bb * 64 + split) * 128 + 16 * g;
#pragma unroll
            for (int q = 0; q < 2; ++q) {
                f32x4 a = Z4, b = Z4;
#pragma unroll
                for (int s = 0; s < 4; ++s) { a = mfma(ckT[2 * q][s], pf[s], a); b = mfma(ckT[2 * q + 1][s], pf[s], b); }
                st16(c, dst + 64 * q, v4u{pack2_tag<T>(a.x, a.y), pack2_tag<T>(a.z, a.w), pack2_tag<T>(b.x, b.y), pack2_tag<T>(b.z, b.w)});
            }
            if (w == 0) {
                const v4u ones = {0x3f803f80u, 0x3f803f80u, 0x3f803f80u, 0x3f803f80u};
                f32x4 l = Z4;
#pragma unroll
                for (int s = 0; s < 4; ++s) l = mfma(ones, pf[s], l);
                if (g == 0) st8(c, WS_ST + ((bb * 32 + h) * 64 + split) * 8, v2u{tagf<T>(M), tagf<T>(l.x)});
            }
            STAMP(4 + 20 * layer);
        }
        v4u wvo[2][4];
        if constexpr (layer == 0) {
            IDS();
            asm volatile("s_waitcnt vmcnt(0)" ::: "memory");
            __syncthreads();
            v2u tr[4][8];
            const int q = n >> 2, pp = n & 3;
#pragma unroll
            for (int t = 0; t < 2; ++t) {
                const int d = 8 * g + 4 * t + q;
#pragma unroll
                for (int nt = 0; nt < 4; ++nt) {
                    const unsigned ad = lds0 + w * 16384 + d * 128 + (((2 * nt + (pp >> 1)) ^ swz8(d)) * 16) + 8 * (pp & 1);
                    TR_ISSUE(tr[nt][0 + t], ad, 0); TR_ISSUE(tr[nt][2 + t], ad, 4096); TR_ISSUE(tr[nt][4 + t], ad, 8192); TR_ISSUE(tr[nt][6 + t], ad, 12288);
                }
            }
            asm volatile("s_waitcnt lgkmcnt(0)" : "+v"(tr[0][0]), "+v"(tr[0][1]), "+v"(tr[0][2]), "+v"(tr[0][3]), "+v"(tr[0][4]), "+v"(tr[0][5]), "+v"(tr[0][6]), "+v"(tr[0][7]),
                         "+v"(tr[1][0]), "+v"(tr[1][1]), "+v"(tr[1][2]), "+v"(tr[1][3]), "+v"(tr[1][4]), "+v"(tr[1][5]), "+v"(tr[1][6]), "+v"(tr[1][7]) :: "memory");
            asm volatile("" : "+v"(tr[2][0]), "+v"(tr[2][1]), "+v"(tr[2][2]), "+v"(tr[2][3]), "+v"(tr[2][4]), "+v"(tr[2][5]), "+v"(tr[2][6]), "+v"(tr[2][7]),
                         "+v"(tr[3][0]), "+v"(tr[3][1]), "+v"(tr[3][2]), "+v"(tr[3][3]), "+v"(tr[3][4]), "+v"(tr[3][5]), "+v"(tr[3][6]), "+v"(tr[3][7]) :: "memory");
#pragma unroll
            for (int ks = 0; ks < 2; ++ks)
#pragma unroll
                for (int ct = 0; ct < 4; ++ct) *(LAS v4u*)(L + w * 16384 + 8192 + (ks * 4 + ct) * 1024 + lane * 16) = wqk[ks][ct];
            const int fs = swz16(n);
#pragma unroll
            for (int ks = 0; ks < 2; ++ks) {
                v4u u0[4], u1[4];
#pragma unroll
                for (int s = 0; s < 4; ++s) {
                    u0[s] = *(LAS v4u*)(L + L_A + (32 * ks + n) * 256 + (((4 * s + g) ^ fs) * 16));
                    u1[s] = *(LAS v4u*)(L + L_A + (32 * ks + 16 + n) * 256 + (((4 * s + g) ^ fs) * 16));
                }
#pragma unroll
                for (int nt = 0; nt < 4; ++nt) {
                    f32x4 a0 = Z4, a1 = Z4;
#pragma unroll
                    for (int s = 0; s < 4; ++s) {
                        const v4u bo = v4u{tr[nt][2 * s].x, tr[nt][2 * s].y, tr[nt][2 * s + 1].x, tr[nt][2 * s + 1].y};
                        a0 = mfma(u0[s], bo, a0); a1 = mfma(u1[s], bo, a1);
                    }
                    *(LAS v4u*)(L + w * 16384 + (ks * 4 + nt) * 1024 + lane * 16) =
                        v4u{pack2(a0.x, a0.y), pack2(a0.z, a0.w), pack2(a1.x, a1.y), pack2(a1.z, a1.w)};
                }
                FENCE();
            }
            __syncthreads();
        }
        STAMP(5 + 20 * layer);
        {
            IDS();
#pragma unroll
            for (int ks = 0; ks < 2; ++ks)
#pragma unroll
                for (int nt = 0; nt < 4; ++nt) wvo[ks][nt] = *(LAS v4u*)(L + w * 16384 + (ks * 4 + nt) * 1024 + lane * 16);
            if constexpr (layer == 1) nap(D_E2);
            const int b = w >> 2, chunk = lane & 7, sp0 = (tid >> 3) & 31;
            const int ib = WS_E2 + (h1 * 8 + j1) * 16384 + (b * 64 + sp0) * 128 + chunk * 16;
            const int sb = WS_ST + ((b * 32 + h1) * 64 + lane) * 8;
            v4u d0, d1; v2u ls;
            int spins = 0;
            for (;;) {
                d0 = ld16(c, ib); d1 = ld16(c, ib + 32 * 128); ls = ld8(c, sb);
                if (__all(okbit<T>(andor<T>(andor<T>(v4<T>(d0), v4<T>(d1)), andor<T>(ls.x, ls.y))))) break;
                if (give_up(c, spins)) break;
            }
            STAMP(6 + 20 * layer);
            LAS unsigned char* im = L + L_A + (b * 64 + sp0) * 128 + ((chunk ^ swz8(sp0)) * 16);
            *(LAS v4u*)im = d0; *(LAS v4u*)(im + 32 * 128) = d1;
            const float Mi = u2f(ls.x);
            const float e = __builtin_amdgcn_exp2f(Mi - wave_max(Mi));
            const float wn = e * __builtin_amdgcn_rcpf(wave_sum(e * u2f(ls.y)));
            v4u wf[2];
            mk_frags<false>(wn, 0.f, L + L_XS + w * 512, lane, wf);
            __syncthreads();
            {
                const int ct = w & 3, q = n >> 2, pp = n & 3;
                v2u t0, t1, t2, t3;
                const int r0 = 8 * g + q;
                const unsigned a0 = lds0 + L_A + (b * 64 + r0) * 128 + (((2 * ct + (pp >> 1)) ^ swz8(r0)) * 16) + 8 * (pp & 1);
                asm volatile("ds_read_b64_tr_b16 %0, %4\n\tds_read_b64_tr_b16 %1, %4 offset:512\n\tds_read_b64_tr_b16 %2, %4 offset:4096\n\tds_read_b64_tr_b16 %3, %4 offset:4608\n\ts_waitcnt lgkmcnt(0)"
                             : "=&v"(t0), "=&v"(t1), "=&v"(t2), "=&v"(t3) : "v"(a0) : "memory");
                f32x4 o = mfma(wf[0], v4u{t0.x, t0.y, t1.x, t1.y}, Z4);
                o = mfma(wf[1], v4u{t2.x, t2.y, t3.x, t3.y}, o);
                const float v = o.x + o.z;
                const float vn = dpp<0xB1>(v);
                const unsigned hi = pack2(v, vn);
                const unsigned lo = pack2(v - bflo(hi), vn - bfhi(hi));
                LAS unsigned* x2 = (LAS unsigned*)(L + L_X2) + b * 32 + 8 * ct + (n >> 1);
                if (lane < 16 && !(lane & 1)) { x2[0] = hi; x2[64] = lo; }
            }
            __syncthreads();
            v4u of[2];
#pragma unroll
            for (int ks = 0; ks < 2; ++ks) { v4u v = {0u, 0u, 0u, 0u}; if (n < 4) v = *(LAS v4u*)(L + L_X2 + n * 128 + 64 * ks + 16 * g); of[ks] = v; }
#pragma unroll
            for (int nt = 0; nt < 4; ++nt) {
                f32x4 a2 = mfma(of[0], wvo[0][nt], Z4);
                a2 = mfma(of[1], wvo[1][nt], a2);
                if (g == 0) st8(c, WS_E3 + (4 * w + nt) * 32768 + cu * 128 + n * 8, v2u{tagf<T>(a2.x + a2.z), tagf<T>(a2.y + a2.w)});
            }
            STAMP(7 + 20 * layer);
            if constexpr (layer == 0) {
                if (idx >= 16 && tid < 112) {
                    const unsigned wv = *(const GAS unsigned*)(p.w2 + (size_t)(112 * (idx - 16) + tid) * MD + 64 * xcd);
                    asm volatile("" ::"v"(wv));
                }
            }
        }
        auto ident = [&](int n, int g, int hi) {
            unsigned a[4];
#pragma unroll
            for (int q = 0; q < 4; ++q) { const int k0 = 8 * g + 2 * q - 16 * hi; a[q] = (k0 == n ? 0x3f80u : 0u) | (k0 + 1 == n ? 0x3f800000u : 0u); }
            return v4u{a[0], a[1], a[2], a[3]};
        };
        if (isRed) {
            IDS();
            nap(D_E3);
            const int ib = WS_E3 + red * 32768 + tid * 16;
            v4u d[4];
            int spins = 0;
            for (;;) {
#pragma unroll
                for (int k = 0; k < 4; ++k) d[k] = ld16(c, ib + k * 8192);
                if (__all(okbit<T>(andor<T>(andor<T>(v4<T>(d[0]), v4<T>(d[1])), andor<T>(v4<T>(d[2]), v4<T>(d[3])))))) break;
                if (give_up(c, spins)) break;
            }
            STAMP(8 + 20 * layer);
            float a[4];
            a[0] = (u2f(d[0].x) + u2f(d[1].x)) + (u2f(d[2].x) + u2f(d[3].x));
            a[1] = (u2f(d[0].y) + u2f(d[1].y)) + (u2f(d[2].y) + u2f(d[3].y));
            a[2] = (u2f(d[0].z) + u2f(d[1].z)) + (u2f(d[2].z) + u2f(d[3].z));
            a[3] = (u2f(d[0].w) + u2f(d[1].w)) + (u2f(d[2].w) + u2f(d[3].w));
#pragma unroll
            for (int i = 0; i < 4; ++i) { a[i] += dpp<0x128>(a[i]); a[i] = x32_sum(x16_sum(a[i])); }
            LAS float* rd = (LAS float*)(L + L_RED);
            if (lane < 8) *(LAS f32x4*)(rd + w * 32 + lane * 4) = f32x4{a[0], a[1], a[2], a[3]};
            __syncthreads();
            if (tid < 32) {
                float v = 0.f;
#pragma unroll
                for (int i = 0; i < 8; ++i) v += rd[i * 32 + tid];
                xres += v;
                st4(c, WS_E4 + red * 128 + tid * 4, tagf<T>(xres));
            }
            STAMP(9 + 20 * layer);
            if constexpr (layer == 0) {
                const u16* pw = p.w2 + (size_t)(224 * w + n) * MD + 32 * (red >> 1) + 8 * g;
                const v4u Isel = ident(n, g, red & 1);
                v4u raw[14];
#pragma unroll
                for (int ft = 0; ft < 14; ++ft) raw[ft] = ldnt(pw + (size_t)ft * 16 * MD);
                FENCE();
#pragma unroll
                for (int ks = 0; ks < 7; ++ks) {
                    const f32x4 a0 = mfma(raw[2 * ks], Isel, Z4), a1 = mfma(raw[2 * ks + 1], Isel, Z4);
                    glf[ks >> 2][ks & 3] = v4u{pack2(a0.x, a0.y), pack2(a0.z, a0.w), pack2(a1.x, a1.y), pack2(a1.z, a1.w)};
                    pin(glf[ks >> 2][ks & 3]);
                }
            }
        }
        if (isGU) {
            IDS();
            if constexpr (layer == 0) {
                const v4u Ilo = ident(n, g, 0), Ihi = ident(n, g, 1);
                v4u raw[2][4];
#pragma unroll
                for (int mt = 0; mt < 4; ++mt) {
                    raw[0][mt] = ldnt(p.w1 + (size_t)(64 * w + 16 * mt + n) * FF + 32 * gu + 8 * g);
                    raw[1][mt] = ldnt(p.w3 + (size_t)(64 * w + 16 * mt + n) * FF + 32 * gu + 8 * g);
                }
                FENCE();
#pragma unroll
                for (int mat = 0; mat < 2; ++mat)
#pragma unroll
                    for (int ks = 0; ks < 2; ++ks) {
                        const f32x4 l0 = mfma(raw[mat][2 * ks], Ilo, Z4), h0 = mfma(raw[mat][2 * ks], Ihi, Z4);
                        const f32x4 l1 = mfma(raw[mat][2 * ks + 1], Ilo, Z4), hh1 = mfma(raw[mat][2 * ks + 1], Ihi, Z4);
                        glf[ks][mat * 2] = v4u{pack2(l0.x, l0.y), pack2(l0.z, l0.w), pack2(l1.x, l1.y), pack2(l1.z, l1.w)};
                        glf[ks][mat * 2 + 1] = v4u{pack2(h0.x, h0.y), pack2(h0.z, h0.w), pack2(hh1.x, hh1.y), pack2(hh1.z, hh1.w)};
                        pin(glf[ks][mat * 2]); pin(glf[ks][mat * 2 + 1]);
                    }
            } else nap(D_E4);
            float x0, x1;
            int spins = 0;
            for (;;) {
                const v2u v = ld8(c, WS_E4 + tid * 8), s = ld8(c, WS_E4);
                x0 = u2f(v.x) - u2f(s.x); x1 = u2f(v.y) - u2f(s.y);
                if (__all(okbit<T>(andor<T>(andor<T>(v.x, v.y), andor<T>(s.x, s.y))))) break;
                if (give_up(c, spins)) break;
            }
            STAMP(10 + 20 * layer);
            ln_gemv_partials(x0, x1, glf, L, w, lane);
            __syncthreads();
            if (tid < 32) {
                float o[4];
                ln_gemv_final(L, tid, o);
                float u[4];
#pragma unroll
                for (int i = 0; i < 4; ++i) { auto r = __builtin_amdgcn_permlane16_swap(f2u(o[i]), f2u(o[i]), false, false); u[i] = u2f(r[1]); }
                if (tid < 16) {
                    float hv[4];
#pragma unroll
                    for (int i = 0; i < 4; ++i) hv[i] = o[i] * __builtin_amdgcn_rcpf(1.0f + __builtin_amdgcn_exp2f(-1.4426950408889634f * o[i])) * u[i];
                    st16(c, WS_E5 + gu * 256 + tid * 16, v4u{tagf<T>(hv[0]), tagf<T>(hv[1]), tagf<T>(hv[2]), tagf<T>(hv[3])});
                }
            }
            STAMP(11 + 20 * layer);
        }
        if (isRed) {
            IDS();
            if constexpr (layer == 1) nap(D_E5);
            const int ib = WS_E5 + (112 * w + lane) * 16;
            const int ib1 = ib + (lane < 48 ? 1024 : 0);
            v4u d0, d1;
            int spins = 0;
            for (;;) {
                d0 = ld16(c, ib); d1 = ld16(c, ib1);
                if (__all(okbit<T>(andor<T>(v4<T>(d0), v4<T>(d1))))) break;
                if (give_up(c, spins)) break;
            }
            STAMP(12 + 20 * layer);
            LAS unsigned char* hs = L + L_A + w * 1856;
#pragma unroll
            for (int k = 0; k < 2; ++k) {
                const v4u d = k ? d1 : d0;
                const int f = 2 * (lane + 64 * k);
                const int pos = (f & ~31) + 8 * ((f >> 2) & 3) + 4 * ((f >> 4) & 1) + (f & 3);
                const unsigned h0 = pack2(u2f(d.x), u2f(d.z)), hb1 = pack2(u2f(d.y), u2f(d.w));
                const unsigned l0 = pack2(u2f(d.x) - bflo(h0), u2f(d.z) - bfhi(h0)), lb1 = pack2(u2f(d.y) - bflo(hb1), u2f(d.w) - bfhi(hb1));
                if (k == 0 || lane < 48) {
                    *(LAS unsigned*)(hs + pos * 2) = h0; *(LAS unsigned*)(hs + 464 + pos * 2) = hb1;
                    *(LAS unsigned*)(hs + 928 + pos * 2) = l0; *(LAS unsigned*)(hs + 1392 + pos * 2) = lb1;
                }
            }
            f32x4 a = Z4, a2 = Z4;
#pragma unroll
            for (int ks = 0; ks < 7; ++ks) {
                v4u v = {0u, 0u, 0u, 0u};
                if (n < 4) v = *(LAS v4u*)(hs + n * 464 + 64 * ks + 16 * g);
                if (ks & 1) a2 = mfma(v, glf[ks >> 2][ks & 3], a2); else a = mfma(v, glf[ks >> 2][ks & 3], a);
            }
            a += a2;
            LAS float* rd = (LAS float*)(L + L_RED);
            if (g == 0) *(LAS f32x2*)(rd + w * 32 + n * 2) = f32x2{a.x + a.z, a.y + a.w};
            __syncthreads();
            if (tid < 32) {
                float v = 0.f;
#pragma unroll
                for (int i = 0; i < 8; ++i) v += rd[i * 32 + tid];
                xres += v;
                if constexpr (layer == 0) st4(c, WS_E6 + red * 128 + tid * 4, tagf<1u>(xres));
                else p.out[(tid & 1) * MD + 16 * red + (tid >> 1)] = (u16)pack2(xres, 0.f);
            }
            STAMP(13 + 20 * layer);
        }
    };
    layer_body(std::integral_constant<int, 0>{});
    layer_body(std::integral_constant<int, 1>{});
    if (lane == 0 && (w == 0 || w == 4)) {
        GAS unsigned* sp = (GAS unsigned*)(p.ws + WS_STAMP) + (cu * 2 + (w >> 2)) * 64;
#pragma unroll
        for (int i = 0; i < 40; ++i) sp[i] = stv[i];
        sp[40] = (unsigned)c.t0; sp[41] = (unsigned)(c.t0 >> 32);
        if (w == 0 && (cu < 10 || cu == 64 || cu == 200)) {
            unsigned xcc; asm volatile("s_getreg_b32 %0, hwreg(HW_REG_XCC_ID)" : "=s"(xcc));
            const unsigned long long t1 = __builtin_amdgcn_s_memrealtime(); const unsigned c1 = (unsigned)__builtin_amdgcn_s_memtime();
            printf("cu %d xcc %u t0 %llu dur %llu cyc %u | wqk %u e1p %u e1g %u e2p %u wvo %u e2g %u e3p %u e6g %u | L1 e1p %u e1g %u e2p %u e2g %u e3p %u e6p %u\n", cu, xcc & 15, c.t0, t1 - c.t0, c1 - stv[0],
                stv[1] - stv[0], stv[2] - stv[0], stv[3] - stv[0], stv[4] - stv[0], stv[5] - stv[0], stv[6] - stv[0], stv[7] - stv[0], stv[20] - stv[0],
                stv[22] - stv[0], stv[23] - stv[0], stv[24] - stv[0], stv[26] - stv[0], stv[27] - stv[0], stv[33] - stv[0]);
        }
    }
    asm volatile("mk_code_end:");
}
}

extern "C" void kernel_launch(void* const* d_in, const int* in_sizes, int n_in,
                              void* d_out, int out_size, void* d_ws, size_t ws_size,
                              hipStream_t stream) {
    mk::Params p;
    p.x = (const u16*)d_in[0]; p.ckv = (const u16*)d_in[1]; p.wq = (const u16*)d_in[2]; p.wuk = (const u16*)d_in[3];
    p.wuv = (const u16*)d_in[4]; p.wo = (const u16*)d_in[5]; p.w1 = (const u16*)d_in[7]; p.w3 = (const u16*)d_in[8];
    p.w2 = (const u16*)d_in[9]; p.out = (u16*)d_out; p.ws = (unsigned char*)d_ws;
    (void)hipMemsetAsync(d_ws, 0, mk::WS_ZERO_BYTES, stream);
    hipLaunchKernelGGL(mk::mega, dim3(mk::NWG), dim3(mk::NT), 0, stream, p);
}
```

```cpp
#include <hip/hip_runtime.h>
#include <hip/hip_bf16.h>
#include <type_traits>

typedef unsigned short u16;
typedef unsigned v4u __attribute__((ext_vector_type(4)));
typedef unsigned v2u __attribute__((ext_vector_type(2)));
typedef float f32x4 __attribute__((ext_vector_type(4)));
typedef float f32x2 __attribute__((ext_vector_type(2)));
typedef short bf16x8 __attribute__((ext_vector_type(8)));
typedef __bf16 bf16x2 __attribute__((ext_vector_type(2)));
#define GAS __attribute__((address_space(1)))
#define LAS __attribute__((address_space(3)))
#define DI __device__ __forceinline__
#define RLX_AGENT __ATOMIC_RELAXED, __HIP_MEMORY_SCOPE_AGENT

#define STAMPS 0
#define WARMPOL "nt"
#define CKV_V 4
#define GW 2
#define CKV_A 4
#define GU_EARLY 1
#define PROF 0

namespace mk {
constexpr int NWG = 256, NT = 512;
constexpr int SK = 8192, DC = 512, MD = 512, HD = 4096, FF = 1792;
constexpr int WS_CTRL = 0;
constexpr int WS_E1 = 256;
constexpr int WS_ST = WS_E1 + 65536;
constexpr int WS_E4 = WS_ST + 32768;
constexpr int WS_E5 = WS_E4 + 4096;
constexpr int WS_E6 = WS_E5 + 14336;
constexpr int WS_ZERO_BYTES = WS_E6 + 4096;
constexpr int WS_E2 = 1 << 20;
constexpr int WS_E3 = WS_E2 + 256 * 16384;
constexpr int WS_STAMP = WS_E3 + 32 * 32768;
constexpr int WS_BYTES = WS_STAMP + 256 * 64 * 8;
static_assert(WS_ZERO_BYTES <= WS_E2 && (WS_ZERO_BYTES % 16) == 0, "ws map");
constexpr int L_A = 131072;
constexpr int L_TQ = L_A + 16384;
constexpr int L_RED = L_A + 17408;
constexpr int L_P = L_RED;
constexpr int L_SMAX = L_RED + 6400;
constexpr int L_XS = L_SMAX + 512;
constexpr int L_X2 = L_XS + 4096;
constexpr int L_CST = L_X2 + 512;
constexpr int L_BYTES = 163840;
static_assert(L_CST + 640 <= L_BYTES && L_CST >= L_A + 7 * 4096, "LDS");

struct Params { const u16 *x, *ckv, *wq, *wuk, *wuv, *wo, *w1, *w3, *w2; u16* out; unsigned char* ws; };

DI float u2f(unsigned u) { return __builtin_bit_cast(float, u); }
DI unsigned f2u(float f) { return __builtin_bit_cast(unsigned, f); }
DI f32x4 mfma(v4u a, v4u b, f32x4 c) {
    return __builtin_amdgcn_mfma_f32_16x16x32_bf16(__builtin_bit_cast(bf16x8, a), __builtin_bit_cast(bf16x8, b), c, 0, 0, 0);
}
DI unsigned pack2(float a, float b) { bf16x2 v = {(__bf16)a, (__bf16)b}; return __builtin_bit_cast(unsigned, v); }
DI float bflo(unsigned u) { return u2f(u << 16); }
DI float bfhi(unsigned u) { return u2f(u & 0xffff0000u); }
template <unsigned T> DI unsigned pack2_tag(float a, float b) {
    const unsigned u = f2u(a);
    const unsigned lo = T ? ((u >> 16) | 1u) : (((u + 0x10000u) >> 17) << 1);
    return __builtin_amdgcn_perm(pack2(b, b), lo, 0x07060100u);
}
template <unsigned T> DI unsigned tagf(float a) { return T ? (f2u(a) | 1u) : (f2u(a) & ~1u); }
DI v4u ldnt(const u16* p) { return __builtin_nontemporal_load((const GAS v4u*)p); }
DI void glds16(const void* gsrc, unsigned lds_dst) {
    unsigned keep;
    asm volatile("s_mov_b32 %0, m0\n\ts_mov_b32 m0, %2\n\ts_nop 0\n\tglobal_load_lds_dwordx4 %1, off nt\n\ts_mov_b32 m0, %0" : "=&s"(keep) : "v"(gsrc), "s"(lds_dst) : "memory");
}
#define LDNT_ASM(dst, ptr) asm volatile("global_load_dwordx4 %0, %1, off nt" : "=v"(dst) : "v"(ptr) : "memory")
DI int swz16(int r) { return ((r >> 3) & 1) | ((r & 3) << 1) | ((((r >> 2) ^ (r >> 3)) & 1) << 3); }
DI int swz8(int r) { return (r & 2) | ((r >> 1) & 4); }
#define TR_ISSUE(dst, addr, OFF) asm volatile("ds_read_b64_tr_b16 %0, %1 offset:" #OFF : "=v"(dst) : "v"(addr) : "memory")

template <int CTRL> DI unsigned dppu(unsigned v) { return (unsigned)__builtin_amdgcn_update_dpp(0, (int)v, CTRL, 0xf, 0xf, true); }
template <int CTRL> DI float dpp(float v) { return u2f(dppu<CTRL>(f2u(v))); }
DI float row_sum16(float v) { v += dpp<0xB1>(v); v += dpp<0x4E>(v); v += dpp<0x141>(v); v += dpp<0x140>(v); return v; }
DI float row_max16(float v) { v = fmaxf(v, dpp<0xB1>(v)); v = fmaxf(v, dpp<0x4E>(v)); v = fmaxf(v, dpp<0x141>(v)); v = fmaxf(v, dpp<0x140>(v)); return v; }
DI float x16_sum(float v) { auto r = __builtin_amdgcn_permlane16_swap(f2u(v), f2u(v), false, false); return u2f(r[0]) + u2f(r[1]); }
DI float x32_sum(float v) { auto r = __builtin_amdgcn_permlane32_swap(f2u(v), f2u(v), false, false); return u2f(r[0]) + u2f(r[1]); }
DI float x16_max(float v) { auto r = __builtin_amdgcn_permlane16_swap(f2u(v), f2u(v), false, false); return fmaxf(u2f(r[0]), u2f(r[1])); }
DI float x32_max(float v) { auto r = __builtin_amdgcn_permlane32_swap(f2u(v), f2u(v), false, false); return fmaxf(u2f(r[0]), u2f(r[1])); }
DI float wave_sum(float v) { return x32_sum(x16_sum(row_sum16(v))); }
DI float wave_max(float v) { return x32_max(x16_max(row_max16(v))); }
DI void pin(v4u& v) { asm volatile("" : "+v"(v)); }
DI void nap(int n) {
#pragma unroll 1
    for (int i = 0; i < n; ++i) __builtin_amdgcn_s_sleep(1);
}

struct Ctx {
    __amdgpu_buffer_rsrc_t rs;
    GAS unsigned* abortw;
    unsigned long long t0;
    bool dead;
};
DI v4u ld16(const Ctx& c, int off) { return __builtin_amdgcn_raw_buffer_load_b128(c.rs, off, 0, 16); }
DI v2u ld8(const Ctx& c, int off) { return __builtin_amdgcn_raw_buffer_load_b64(c.rs, off, 0, 16); }
DI void st16(const Ctx& c, int off, v4u v) { __builtin_amdgcn_raw_buffer_store_b128(v, c.rs, off, 0, 16); }
DI void st8(const Ctx& c, int off, v2u v) { __builtin_amdgcn_raw_buffer_store_b64(v, c.rs, off, 0, 16); }
DI void st4(const Ctx& c, int off, unsigned v) { __builtin_amdgcn_raw_buffer_store_b32(v, c.rs, off, 0, 16); }
DI bool give_up(Ctx& c, int& spins) {
    if (c.dead) return true;
    if ((++spins & 63) == 0) {
        const bool late = (long long)(__builtin_amdgcn_s_memrealtime() - c.t0) > 5000000ll;
        if (late || __hip_atomic_load(c.abortw, RLX_AGENT) != 0u) { __hip_atomic_store(c.abortw, 1u, RLX_AGENT); c.dead = true; return true; }
    }
    return false;
}
template <unsigned T> DI unsigned andor(unsigned a, unsigned b) { return T ? (a & b) : (a | b); }
template <unsigned T> DI unsigned v4(v4u v) { return andor<T>(andor<T>(v.x, v.y), andor<T>(v.z, v.w)); }
template <unsigned T> DI bool okbit(unsigned m) { return (m & 1u) == T; }

DI void pini(int& v) { asm volatile("" : "+v"(v)); }
#define PP_GAP 0
template <class D, class LD, class OK> DI void poll2(Ctx& c, D& out, LD ld, OK ok) {
    int spins = 0;
    for (;;) {
        ld(out);
        if (__all(ok(out))) return;
        if (give_up(c, spins)) return;
    }
}
struct D2x8 { v2u v, s; };
struct D4x16 { v4u d[4]; };
struct D2x16 { v4u d0, d1; };
struct FragAddr { int wr, rd; };
template <bool PERM> DI FragAddr frag_addr(int w, int lane) {
    const int n = lane & 15, g = lane >> 4;
    const int pos2 = PERM ? (16 * (lane >> 5) + 4 * ((lane >> 2) & 3) + 2 * ((lane >> 4) & 1) + ((lane >> 1) & 1)) : (lane >> 1);
    FragAddr f;
    f.wr = L_XS + w * 512 + ((lane & 1) ? 256 : 0) + pos2 * 4;
    f.rd = (n < 4 ? L_XS + w * 512 + n * 128 : (n == 4 ? L_CST : L_CST + 128)) + 16 * g;
    pini(f.wr); pini(f.rd);
    return f;
}
DI void mk_frags(float v0, float v1, LAS unsigned char* L, const FragAddr& f, bool odd, v4u (&fr)[2]) {
    const unsigned hi = pack2(v0, v1);
    const unsigned lo = pack2(v0 - bflo(hi), v1 - bfhi(hi));
    const unsigned recv = dppu<0xB1>(odd ? hi : lo);
    const unsigned a = odd ? recv : hi, b = odd ? lo : recv;
    LAS unsigned* x32 = (LAS unsigned*)(L + f.wr);
    x32[0] = __builtin_amdgcn_perm(b, a, 0x05040100u); x32[32] = __builtin_amdgcn_perm(b, a, 0x07060302u);
    fr[0] = *(LAS v4u*)(L + f.rd); fr[1] = *(LAS v4u*)(L + f.rd + 64);
}
DI float sum8(float v) { v += dpp<0xB1>(v); v += dpp<0x4E>(v); v += dpp<0x141>(v); return v; }

#define IDS() int tid = threadIdx.x; asm volatile("" : "+v"(tid)); const int lane = tid & 63, n = lane & 15, g = lane >> 4; (void)n; (void)g; (void)lane
#define FENCE() __builtin_amdgcn_sched_barrier(0)
#define STAMP(i) do {} while (0)

#define D_E1A 0
#define D_E1 16
#define D_E2 40
#define D_E3 20
#define D_E4 70
#define D_E5 50
#define D_E6 130
#define D_E6R 14
#define D_E6G 50

struct GemvAddr { FragAddr f; int wq, ws, rq, rc, rs; };
DI GemvAddr gemv_addr(int w, int lane, int pair  ) {
    const int n = lane & 15, g = lane >> 4;
    GemvAddr a;
    a.f = frag_addr<true>(w, lane);
    a.wq = L_RED + w * 768 + (g == 0 ? n * 8 : 512 + n * 4);
    a.ws = L_RED + 6144 + w * 16 + (lane == 4 ? 0 : 8 + lane * 4);
    a.rq = L_RED + (lane & 7) * 768 + pair * 16;
    a.rc = L_RED + (lane & 7) * 768 + 512 + pair * 8;
    a.rs = L_RED + 6144 + (lane & 7) * 16;
    pini(a.wq); pini(a.ws); pini(a.rq); pini(a.rc); pini(a.rs);
    return a;
}
DI void ln_gemv_partials(float x0, float x1, const v4u (&bf)[2][4], LAS unsigned char* L, const GemvAddr& A, int lane) {
    const int g = lane >> 4;
    v4u xf[2];
    mk_frags(x0, x1, L, A.f, lane & 1, xf);
    const f32x4 Z = {0.f, 0.f, 0.f, 0.f};
    f32x4 a[4], gm;
#pragma unroll
    for (int ct = 0; ct < 4; ++ct) a[ct] = mfma(xf[0], bf[0][ct], Z);
    gm = mfma(xf[0], xf[0], Z);
#pragma unroll
    for (int ct = 0; ct < 4; ++ct) a[ct] = mfma(xf[1], bf[1][ct], a[ct]);
    gm = mfma(xf[1], xf[1], gm);
    if (g == 0) {
#pragma unroll
        for (int ct = 0; ct < 4; ++ct) *(LAS f32x2*)(L + A.wq + ct * 128) = f32x2{a[ct].x + a[ct].z, a[ct].y + a[ct].w};
        if (lane == 4) *(LAS f32x2*)(L + A.ws) = f32x2{gm.x + gm.z, gm.y + gm.w};
        if (lane < 2) *(LAS float*)(L + A.ws) = lane == 0 ? gm.x + 2.f * gm.z : gm.y + 2.f * gm.w;
    } else if (g == 1) {
#pragma unroll
        for (int ct = 0; ct < 4; ++ct) *(LAS float*)(L + A.wq + ct * 64) = a[ct].x;
    }
}
DI void ln_gemv_final(LAS unsigned char* L, const GemvAddr& A, float (&o)[4]) {
    f32x4 q = *(LAS f32x4*)(L + A.rq), st = *(LAS f32x4*)(L + A.rs);
    f32x2 cs = *(LAS f32x2*)(L + A.rc);
    st.x = sum8(st.x); st.y = sum8(st.y); st.z = sum8(st.z); st.w = sum8(st.w);
    q.x = sum8(q.x); q.y = sum8(q.y); q.z = sum8(q.z); q.w = sum8(q.w);
    cs.x = sum8(cs.x); cs.y = sum8(cs.y);
    const float m0 = st.x * (1.f / 512), m1 = st.y * (1.f / 512);
    const float r0 = __builtin_amdgcn_rsqf(fmaxf(st.z * (1.f / 512) - m0 * m0, 0.f) + 1e-5f);
    const float r1 = __builtin_amdgcn_rsqf(fmaxf(st.w * (1.f / 512) - m1 * m1, 0.f) + 1e-5f);
    o[0] = (q.x - m0 * cs.x) * r0; o[1] = (q.y - m1 * cs.x) * r1;
    o[2] = (q.z - m0 * cs.y) * r0; o[3] = (q.w - m1 * cs.y) * r1;
}

__global__ void __launch_bounds__(NT, 2) mega(Params p) {
    __shared__ __attribute__((aligned(16))) unsigned char smem[L_BYTES];
    LAS unsigned char* L = (LAS unsigned char*)smem;
    const int tid = threadIdx.x, lane = tid & 63, w = __builtin_amdgcn_readfirstlane(tid >> 6), n = lane & 15, g = lane >> 4;
    const int cu = blockIdx.x, xcd = cu & 7, idx = cu >> 3;
    const int h1 = xcd * 4 + (idx >> 3), j1 = idx & 7;
    const int gs = xcd * 16 + ((idx & 7) | ((idx >> 4) << 3)), bb = gs >> 6, split = gs & 63, hg = 1 - ((idx >> 3) & 1);
    const bool isRed = idx < 4, isGU = idx >= 8 && idx < 15;
    const int red = xcd * 4 + idx, gu = xcd * 7 + (idx - 8);
    unsigned cwarm = 0;
    {
        unsigned long long pc; unsigned len;
        asm volatile("mk_code_begin:\n\ts_getpc_b64 %0\n\ts_mov_b32 %1, mk_code_end-mk_code_begin" : "=s"(pc), "=s"(len));
        const unsigned long long ca = pc + (((unsigned)tid * 128u < len) ? (unsigned)tid * 128u : 0u);
        asm volatile("global_load_dword %0, %1, off" : "=v"(cwarm) : "v"(ca) : "memory");
    }
    Ctx c;
    c.rs = __builtin_amdgcn_make_buffer_rsrc((void*)p.ws, 0, WS_BYTES, 0x00020000);
    c.abortw = (GAS unsigned*)(p.ws + WS_CTRL);
    c.t0 = __builtin_amdgcn_s_memrealtime();
    c.dead = false;
    STAMP(0);
    const f32x4 Z4 = {0.f, 0.f, 0.f, 0.f};

    v4u ck[16];
    v4u ckT[4][4];
    v4u glf[2][4];
    const u16* ckv0 = p.ckv + ((size_t)bb * SK + (size_t)split * 128) * DC;
    unsigned xr0, xr1, xr2, xr3, xs0, xs1;
    {
        const u16* px = p.x + tid;
        const u16* pr = p.x + (16 * (red & 31) + 2 * (lane >> 3));
        asm volatile("global_load_ushort %0, %6, off\n\tglobal_load_ushort %1, %6, off offset:1024\n\tglobal_load_dword %2, %7, off\n\tglobal_load_dword %3, %7, off offset:1024\n\t"
                     "global_load_ushort %4, %8, off\n\tglobal_load_ushort %5, %8, off offset:1024"
                     : "=&v"(xr0), "=&v"(xr1), "=&v"(xr2), "=&v"(xr3), "=&v"(xs0), "=&v"(xs1) : "v"(px), "v"(pr), "v"(p.x) : "memory");
    }

    v4u wqk[2][4];
    const unsigned lds0 = (unsigned)(size_t)smem;
    auto ckv_rows = [&](int i0 = 0, int i1 = 16) {
#pragma unroll 4
        for (int i = i0; i < i1; ++i) glds16(ckv0 + (size_t)(16 * w + i) * DC + ((lane ^ swz16(i)) * 8), lds0 + (16 * w + i) * 1024);
    };
    {
        v4u aq[4][4];
#pragma unroll
        for (int i = 0; i < 2; ++i) {
            const int r = 8 * w + 4 * i + g;
            glds16(p.wuk + (size_t)(64 * j1 + r) * HD + h1 * 128 + ((n ^ swz16(r & 15)) * 8), lds0 + L_A + (8 * w + 4 * i) * 256);
        }
        {
            const u16* pq = p.wq + (size_t)(64 * w + g) * HD + h1 * 128 + 8 * n;
#pragma unroll
            for (int mt = 0; mt < 4; ++mt)
#pragma unroll
                for (int i = 0; i < 4; ++i) { const u16* pp = pq + (size_t)(16 * mt + 4 * i) * HD; LDNT_ASM(aq[mt][i], pp); }
        }
        asm volatile("s_waitcnt vmcnt(16)" : "+v"(xr0), "+v"(xr1), "+v"(xr2), "+v"(xr3), "+v"(xs0), "+v"(xs1), "+v"(cwarm) :: "memory");
        STAMP(14);
        __syncthreads();
        STAMP(15);
        const int fs = swz16(n);
        v4u bu[4][4];
#pragma unroll
        for (int ct = 0; ct < 4; ++ct)
#pragma unroll
            for (int s = 0; s < 4; ++s) bu[ct][s] = *(LAS v4u*)(L + L_A + (16 * ct + n) * 256 + (((4 * s + g) ^ fs) * 16));
#pragma unroll
        for (int ct = 0; ct < 4; ++ct)
#pragma unroll
            for (int s = 0; s < 4; ++s) pin(bu[ct][s]);
        __syncthreads();
        STAMP(16);
        if (w != 7) ckv_rows(0, CKV_A);
        else {
            const v4u z = {0u, 0u, 0u, 0u};
            const int ib = WS_E2 + (h1 * 8 + j1) * 16384 + lane * 16;
#pragma unroll
            for (int i = 0; i < 16; ++i) st16(c, ib + i * 1024, z);
#pragma unroll
            for (int i = 0; i < 4; ++i) st16(c, WS_E3 + cu * 4096 + lane * 16 + i * 1024, z);
        }
        STAMP(17);
#define WAITQ(NA, NB, mt) asm volatile("s_cmp_lt_u32 %4, 7\n\ts_cbranch_scc1 1f\n\ts_waitcnt vmcnt(" #NB ")\n\ts_branch 2f\n1:\n\ts_waitcnt vmcnt(%5)\n2:" \
            : "+v"(aq[mt][0]), "+v"(aq[mt][1]), "+v"(aq[mt][2]), "+v"(aq[mt][3]) : "s"(w), "n"(NA + CKV_A) : "memory", "scc")
        LAS unsigned char* tq = L + L_A + w * 4096;
#pragma unroll
        for (int mt = 0; mt < 4; ++mt) {
            if (mt == 0) WAITQ(12, 32, 0);
            else if (mt == 1) WAITQ(8, 28, 1);
            else if (mt == 2) WAITQ(4, 24, 2);
            else { WAITQ(0, 0, 3); STAMP(18); }
#pragma unroll
            for (int i = 0; i < 4; ++i) { const int r = 4 * i + g; *(LAS v4u*)(tq + r * 256 + ((n ^ swz16(r)) * 16)) = aq[mt][i]; }
            v4u af[4];
#pragma unroll
            for (int s = 0; s < 4; ++s) af[s] = *(LAS v4u*)(tq + n * 256 + (((4 * s + g) ^ fs) * 16));
#pragma unroll
            for (int ct = 0; ct < 4; ++ct) {
                f32x4 a0 = Z4;
#pragma unroll
                for (int s = 0; s < 4; ++s) a0 = mfma(af[s], bu[ct][s], a0);
                if (mt & 1) { wqk[mt >> 1][ct].z = pack2(a0.x, a0.y); wqk[mt >> 1][ct].w = pack2(a0.z, a0.w); }
                else { wqk[mt >> 1][ct].x = pack2(a0.x, a0.y); wqk[mt >> 1][ct].y = pack2(a0.z, a0.w); }
            }
        }
        if (w != 7) ckv_rows(CKV_A, 16); else ckv_rows();
#pragma unroll
        for (int ks = 0; ks < 2; ++ks)
#pragma unroll
            for (int ct = 0; ct < 4; ++ct) pin(wqk[ks][ct]);
    }
    if (w == 7 && lane < 40) *(LAS v4u*)(L + L_CST + lane * 16) = lane < 8 ? v4u{0x3f803f80u, 0x3f803f80u, 0x3f803f80u, 0x3f803f80u} : v4u{0u, 0u, 0u, 0u};
    __syncthreads();
    STAMP(1);
    f32x4 xres = {bflo(xr2), bflo(xr3), bfhi(xr2), bfhi(xr3)};

    auto wo_rows = [&](int v) {
        const int rr = lane >> 3;
#pragma unroll 4
        for (int i = 0; i < 16; ++i) {
            const int d = 8 * i + rr;
            glds16(p.wo + (size_t)(h1 * 128 + d) * MD + 64 * v + (((lane & 7) ^ swz8(d)) * 8), lds0 + v * 16384 + i * 1024);
        }
    };
    unsigned warm = 0;
    auto layer_body = [&](auto LC) {
        constexpr int layer = decltype(LC)::value;
        constexpr unsigned T = layer == 0 ? 1u : 0u;
        {
            IDS();
            const int pair = (tid >> 3) & 31;
            const GemvAddr GA = gemv_addr(w, lane, pair);
            int dst = WS_E1 + ((h1 & 1) * 16 + (h1 >> 1)) * 1024 + (64 * j1 + 2 * pair) * 2;
            pini(dst);
            float x0, x1;
            if constexpr (layer == 0) { x0 = bflo(xr0) - bflo(xs0); x1 = bflo(xr1) - bflo(xs1); }
            else {
#pragma unroll
                for (int ks = 0; ks < 2; ++ks)
#pragma unroll
                    for (int ct = 0; ct < 4; ++ct) wqk[ks][ct] = *(LAS v4u*)(L + w * 16384 + 8192 + (ks * 4 + ct) * 1024 + lane * 16);
                nap(isRed ? D_E6R : (isGU ? D_E6G : D_E6));
                D2x8 dd;
                poll2(c, dd, [&](D2x8& d) { d.v = ld8(c, WS_E6 + tid * 8); d.s = ld8(c, WS_E6); }, [&](const D2x8& d) { return okbit<1u>(d.v.x & d.v.y & d.s.x & d.s.y); });
                x0 = u2f(dd.v.x) - u2f(dd.s.x); x1 = u2f(dd.v.y) - u2f(dd.s.y);
                STAMP(20);
            }
            ln_gemv_partials(x0, x1, wqk, L, GA, lane);
            __syncthreads();
            if (w < 4) {
                float o[4];
                ln_gemv_final(L, GA, o);
                const float SC = 0.08838834764831845f * 1.4426950408889634f;
                if ((lane & 7) == 0) {
                    st4(c, dst, pack2_tag<T>(o[0] * SC, o[2] * SC));
                    st4(c, dst + 32768, pack2_tag<T>(o[1] * SC, o[3] * SC));
                }
            }
            STAMP(2 + 20 * layer);
        }
        v4u wuvr[2];
        {
            IDS();
            if constexpr (layer == 0) {
                {
                    const u16* pu = p.wuv + (size_t)(64 * j1 + (tid >> 4)) * HD + h1 * 128 + (tid & 15) * 8;
                    const u16* pu2 = pu + (size_t)32 * HD;
                    LDNT_ASM(wuvr[0], pu); LDNT_ASM(wuvr[1], pu2);
                }
                asm volatile("s_waitcnt vmcnt(2)" ::: "memory");
                STAMP(19);
                __syncthreads();
                const int fs = swz16(n);
#pragma unroll
                for (int s = 0; s < 16; ++s) ck[s] = *(LAS v4u*)(L + (16 * w + n) * 1024 + (((4 * s + g) ^ fs) * 16));
                v2u tr[4][8];
                const int q = n >> 2, pp = n & 3;
#pragma unroll
                for (int t = 0; t < 2; ++t) {
                    const int r = 8 * g + 4 * t + q;
#pragma unroll
                    for (int ct = 0; ct < 4; ++ct) {
                        const unsigned ad = lds0 + r * 1024 + (((8 * w + 2 * ct + (pp >> 1)) ^ swz16(r & 15)) * 16) + 8 * (pp & 1);
                        TR_ISSUE(tr[ct][0 + t], ad, 0); TR_ISSUE(tr[ct][2 + t], ad, 32768);
                        const unsigned ad2 = ad + 65536;
                        TR_ISSUE(tr[ct][4 + t], ad2, 0); TR_ISSUE(tr[ct][6 + t], ad2, 32768);
                    }
                }
                asm volatile("s_waitcnt lgkmcnt(0)" : "+v"(tr[0][0]), "+v"(tr[0][1]), "+v"(tr[0][2]), "+v"(tr[0][3]), "+v"(tr[0][4]), "+v"(tr[0][5]), "+v"(tr[0][6]), "+v"(tr[0][7]),
                             "+v"(tr[1][0]), "+v"(tr[1][1]), "+v"(tr[1][2]), "+v"(tr[1][3]), "+v"(tr[1][4]), "+v"(tr[1][5]), "+v"(tr[1][6]), "+v"(tr[1][7]) :: "memory");
                asm volatile("" : "+v"(tr[2][0]), "+v"(tr[2][1]), "+v"(tr[2][2]), "+v"(tr[2][3]), "+v"(tr[2][4]), "+v"(tr[2][5]), "+v"(tr[2][6]), "+v"(tr[2][7]),
                             "+v"(tr[3][0]), "+v"(tr[3][1]), "+v"(tr[3][2]), "+v"(tr[3][3]), "+v"(tr[3][4]), "+v"(tr[3][5]), "+v"(tr[3][6]), "+v"(tr[3][7]) :: "memory");
#pragma unroll
                for (int ct = 0; ct < 4; ++ct)
#pragma unroll
                    for (int s = 0; s < 4; ++s) ckT[ct][s] = v4u{tr[ct][2 * s].x, tr[ct][2 * s].y, tr[ct][2 * s + 1].x, tr[ct][2 * s + 1].y};
#pragma unroll
                for (int s = 0; s < 16; ++s) pin(ck[s]);
                __syncthreads();
                STAMP(17);
                if (w >= 4) wo_rows(w - 4);
            }
            if (w < 4) {
                const int t = tid;
                v4u qg[4];
                const int e1 = WS_E1 + (bb * 2 + hg) * 16384 + t * 16;
                nap(layer == 0 ? D_E1A : D_E1);
                D4x16 dd;
                poll2(c, dd, [&](D4x16& d) {
#pragma unroll
                    for (int k = 0; k < 4; ++k) d.d[k] = ld16(c, e1 + k * 4096); },
                    [&](const D4x16& d) { return okbit<T>(andor<T>(andor<T>(v4<T>(d.d[0]), v4<T>(d.d[1])), andor<T>(v4<T>(d.d[2]), v4<T>(d.d[3])))); });
#pragma unroll
                for (int k = 0; k < 4; ++k) qg[k] = dd.d[k];
#pragma unroll
                for (int k = 0; k < 4; ++k) { const int u = t + 256 * k; *(LAS v4u*)(L + L_A + (u >> 6) * 1040 + (u & 63) * 16) = qg[k]; }
            }
        }
        {
            IDS();
            int e2a = WS_E2 + ((2 * (n & 7) + hg) * 8 + w) * 16384 + (bb * 64 + split) * 128 + 16 * g + (n < 8 ? 0 : 64);
            int e2b = WS_E2 + ((2 * ((n & 7) + 8) + hg) * 8 + w) * 16384 + (bb * 64 + split) * 128 + 16 * g + (n < 8 ? 64 : 0);
            int qab = L_A + n * 1040 + 16 * g, smw = L_SMAX + (n * 8 + w) * 4, smr = L_SMAX + n * 32;
            int pw_ = L_P + n * 272 + (16 * w + 4 * g) * 2, pr_ = L_P + n * 272 + 16 * g;
            pini(qab); pini(e2a); pini(e2b); pini(smw); pini(smr); pini(pw_); pini(pr_);
            __syncthreads();
            STAMP(3 + 20 * layer);
            f32x4 s0 = Z4, s1 = Z4;
            {
                v4u q[8];
#pragma unroll
                for (int s = 0; s < 8; ++s) q[s] = *(LAS v4u*)(L + qab + 64 * s);
                FENCE();
#pragma unroll
                for (int s = 0; s < 8; s += 2) {
                    s0 = mfma(ck[s], q[s], s0); s1 = mfma(ck[s + 1], q[s + 1], s1);
                    q[s] = *(LAS v4u*)(L + qab + 64 * (s + 8)); q[s + 1] = *(LAS v4u*)(L + qab + 64 * (s + 9));
                }
#pragma unroll
                for (int s = 0; s < 8; s += 2) { s0 = mfma(ck[s + 8], q[s], s0); s1 = mfma(ck[s + 9], q[s + 1], s1); }
            }
            s0 += s1;
            float mx = fmaxf(fmaxf(s0.x, s0.y), fmaxf(s0.z, s0.w));
            mx = x32_max(x16_max(mx));
            if (g == 0) *(LAS float*)(L + smw) = mx;
            __syncthreads();
            if constexpr (layer == 0) {
                asm volatile("s_cmp_lt_u32 %2, 4\n\ts_cbranch_scc1 1f\n\ts_waitcnt vmcnt(16)\n\ts_branch 2f\n1:\n\ts_waitcnt vmcnt(0)\n2:" : "+v"(wuvr[0]), "+v"(wuvr[1]) : "s"(w) : "memory", "scc");
                const int r = tid >> 4;
                LAS unsigned char* dst = L + L_A + r * 256 + (((tid & 15) ^ swz16(r & 15)) * 16);
                *(LAS v4u*)dst = wuvr[0]; *(LAS v4u*)(dst + 32 * 256) = wuvr[1];
            }
            const f32x4 ma = *(LAS f32x4*)(L + smr), mb = *(LAS f32x4*)(L + smr + 16);
            const float M = fmaxf(fmaxf(fmaxf(ma.x, ma.y), fmaxf(ma.z, ma.w)), fmaxf(fmaxf(mb.x, mb.y), fmaxf(mb.z, mb.w)));
            const unsigned p01 = pack2(__builtin_amdgcn_exp2f(s0.x - M), __builtin_amdgcn_exp2f(s0.y - M));
            const unsigned p23 = pack2(__builtin_amdgcn_exp2f(s0.z - M), __builtin_amdgcn_exp2f(s0.w - M));
            *(LAS v2u*)(L + pw_) = v2u{p01, p23};
            if constexpr (layer == 0) asm volatile("s_waitcnt vmcnt(0)" ::: "memory");
            __syncthreads();
            v4u pf[4];
#pragma unroll
            for (int s = 0; s < 4; ++s) pf[s] = *(LAS v4u*)(L + pr_ + 64 * s);
            const int h = 2 * n + hg;
            v4u ov[2];
#pragma unroll
            for (int q = 0; q < 2; ++q) {
                f32x4 a = Z4, b = Z4;
#pragma unroll
                for (int s = 0; s < 4; ++s) { a = mfma(ckT[2 * q][s], pf[s], a); b = mfma(ckT[2 * q + 1][s], pf[s], b); }
                ov[q] = v4u{pack2_tag<T>(a.x, a.y), pack2_tag<T>(a.z, a.w), pack2_tag<T>(b.x, b.y), pack2_tag<T>(b.z, b.w)};
            }
            {
                const v4u r1 = {dppu<0x128>(ov[1].x), dppu<0x128>(ov[1].y), dppu<0x128>(ov[1].z), dppu<0x128>(ov[1].w)};
                const bool lo8 = n < 8;
                st16(c, e2a, lo8 ? ov[0] : r1);
                st16(c, e2b, lo8 ? r1 : ov[0]);
            }
            if (w == 0) {
                const v4u ones = {0x3f803f80u, 0x3f803f80u, 0x3f803f80u, 0x3f803f80u};
                f32x4 l = Z4;
#pragma unroll
                for (int s = 0; s < 4; ++s) l = mfma(ones, pf[s], l);
                if (g == 0) st8(c, WS_ST + ((bb * 32 + h) * 64 + split) * 8, v2u{tagf<T>(M), tagf<T>(l.x)});
            }
            STAMP(4 + 20 * layer);
            if constexpr (layer == 0) {
                if (w >= 4) wo_rows(w);
            }
        }
        v4u wvo[2][4];
        if constexpr (layer == 0) {
            IDS();
            if (w >= 4) asm volatile("s_waitcnt vmcnt(0)" ::: "memory");
            v2u tr[4][8];
            const int q = n >> 2, pp = n & 3;
#pragma unroll
            for (int t = 0; t < 2; ++t) {
                const int d = 8 * g + 4 * t + q;
#pragma unroll
                for (int nt = 0; nt < 4; ++nt) {
                    const unsigned ad = lds0 + w * 16384 + d * 128 + (((2 * nt + (pp >> 1)) ^ swz8(d)) * 16) + 8 * (pp & 1);
                    TR_ISSUE(tr[nt][0 + t], ad, 0); TR_ISSUE(tr[nt][2 + t], ad, 4096); TR_ISSUE(tr[nt][4 + t], ad, 8192); TR_ISSUE(tr[nt][6 + t], ad, 12288);
                }
            }
            asm volatile("s_waitcnt lgkmcnt(0)" : "+v"(tr[0][0]), "+v"(tr[0][1]), "+v"(tr[0][2]), "+v"(tr[0][3]), "+v"(tr[0][4]), "+v"(tr[0][5]), "+v"(tr[0][6]), "+v"(tr[0][7]),
                         "+v"(tr[1][0]), "+v"(tr[1][1]), "+v"(tr[1][2]), "+v"(tr[1][3]), "+v"(tr[1][4]), "+v"(tr[1][5]), "+v"(tr[1][6]), "+v"(tr[1][7]) :: "memory");
            asm volatile("" : "+v"(tr[2][0]), "+v"(tr[2][1]), "+v"(tr[2][2]), "+v"(tr[2][3]), "+v"(tr[2][4]), "+v"(tr[2][5]), "+v"(tr[2][6]), "+v"(tr[2][7]),
                         "+v"(tr[3][0]), "+v"(tr[3][1]), "+v"(tr[3][2]), "+v"(tr[3][3]), "+v"(tr[3][4]), "+v"(tr[3][5]), "+v"(tr[3][6]), "+v"(tr[3][7]) :: "memory");
#pragma unroll
            for (int ks = 0; ks < 2; ++ks)
#pragma unroll
                for (int ct = 0; ct < 4; ++ct) *(LAS v4u*)(L + w * 16384 + 8192 + (ks * 4 + ct) * 1024 + lane * 16) = wqk[ks][ct];
            const int fs = swz16(n);
#pragma unroll
            for (int ks = 0; ks < 2; ++ks) {
                v4u u0[4], u1[4];
#pragma unroll
                for (int s = 0; s < 4; ++s) {
                    u0[s] = *(LAS v4u*)(L + L_A + (32 * ks + n) * 256 + (((4 * s + g) ^ fs) * 16));
                    u1[s] = *(LAS v4u*)(L + L_A + (32 * ks + 16 + n) * 256 + (((4 * s + g) ^ fs) * 16));
                }
#pragma unroll
                for (int nt = 0; nt < 4; ++nt) {
                    f32x4 a0 = Z4, a1 = Z4;
#pragma unroll
                    for (int s = 0; s < 4; ++s) {
                        const v4u bo = v4u{tr[nt][2 * s].x, tr[nt][2 * s].y, tr[nt][2 * s + 1].x, tr[nt][2 * s + 1].y};
                        a0 = mfma(u0[s], bo, a0); a1 = mfma(u1[s], bo, a1);
                    }
                    *(LAS v4u*)(L + w * 16384 + (ks * 4 + nt) * 1024 + lane * 16) =
                        v4u{pack2(a0.x, a0.y), pack2(a0.z, a0.w), pack2(a1.x, a1.y), pack2(a1.z, a1.w)};
                }
                FENCE();
            }
            if (w < 4) {
                    const u16* pb = tid < 56 ? p.w2 + (size_t)(56 * idx + tid) * MD + 64 * xcd
                                             : ((tid < 136 ? p.w1 : p.w3) + (size_t)(16 * idx + ((tid - 56) % 80) / 5) * FF + min(224 * xcd + 64 * (((tid - 56) % 80) % 5), FF - 2));
                    asm volatile("global_load_dword %0, %1, off" : "=v"(warm) : "v"(pb) : "memory");
            }
            __syncthreads();
        }
        STAMP(5 + 20 * layer);
        {
            IDS();
#pragma unroll
            for (int ks = 0; ks < 2; ++ks)
#pragma unroll
                for (int nt = 0; nt < 4; ++nt) wvo[ks][nt] = *(LAS v4u*)(L + w * 16384 + (ks * 4 + nt) * 1024 + lane * 16);
            if constexpr (layer == 1) nap(D_E2);
            const int b = w >> 2, chunk = lane & 7, sp0 = (tid >> 3) & 31;
            const int ib = WS_E2 + (h1 * 8 + j1) * 16384 + (b * 64 + sp0) * 128 + chunk * 16;
            const int sb = WS_ST + ((b * 32 + h1) * 64 + lane) * 8;
            const FragAddr FA = frag_addr<false>(w, lane);
            int imo = L_A + (b * 64 + sp0) * 128 + ((chunk ^ swz8(sp0)) * 16);
            int tra = (int)lds0 + L_A + (b * 64 + 8 * g + (n >> 2)) * 128 + (((2 * (w & 3) + ((n & 3) >> 1)) ^ swz8(8 * g + (n >> 2))) * 16) + 8 * (n & 1);
            int x2w = L_X2 + (b * 32 + 8 * (w & 3) + (n >> 1)) * 4;
            int ofr = (n < 4 ? L_X2 + n * 128 : L_CST + 128) + 16 * g;
            int e3d = WS_E3 + 4 * w * 32768 + cu * 128 + n * 8;
            pini(imo); pini(tra); pini(x2w); pini(ofr); pini(e3d);
            v4u d0, d1; v2u ls;
            int spins = 0;
            for (;;) {
                d0 = ld16(c, ib); d1 = ld16(c, ib + 32 * 128); ls = ld8(c, sb);
                if (__all(okbit<T>(andor<T>(andor<T>(v4<T>(d0), v4<T>(d1)), andor<T>(ls.x, ls.y))))) break;
                if (give_up(c, spins)) break;
            }
            STAMP(6 + 20 * layer);
            if constexpr (layer == 0) {
                asm volatile("" ::"v"(warm));
                const int gq = isGU ? gu : 0;
#pragma unroll
                for (int mt = 0; mt < 4; ++mt) {
                    const u16* pa = p.w1 + (size_t)(64 * w + 16 * mt + n) * FF + 32 * gq + 8 * g;
                    const u16* pb = p.w3 + (size_t)(64 * w + 16 * mt + n) * FF + 32 * gq + 8 * g;
                    if (isGU) { glf[0][mt] = ldnt(pa); glf[1][mt] = ldnt(pb); }
                }
                FENCE();
            }
            *(LAS v4u*)(L + imo) = d0; *(LAS v4u*)(L + imo + 32 * 128) = d1;
            const float Mi = u2f(ls.x);
            const float e = __builtin_amdgcn_exp2f(Mi - wave_max(Mi));
            const float wn = e * __builtin_amdgcn_rcpf(wave_sum(e * u2f(ls.y)));
            v4u wf[2];
            mk_frags(wn, 0.f, L, FA, lane & 1, wf);
            __syncthreads();
            {
                v2u t0, t1, t2, t3;
                asm volatile("ds_read_b64_tr_b16 %0, %4\n\tds_read_b64_tr_b16 %1, %4 offset:512\n\tds_read_b64_tr_b16 %2, %4 offset:4096\n\tds_read_b64_tr_b16 %3, %4 offset:4608\n\ts_waitcnt lgkmcnt(0)"
                             : "=&v"(t0), "=&v"(t1), "=&v"(t2), "=&v"(t3) : "v"(tra) : "memory");
                f32x4 o = mfma(wf[0], v4u{t0.x, t0.y, t1.x, t1.y}, Z4);
                o = mfma(wf[1], v4u{t2.x, t2.y, t3.x, t3.y}, o);
                const float v = o.x + o.z;
                const float vn = dpp<0xB1>(v);
                const unsigned hi = pack2(v, vn);
                const unsigned lo = pack2(v - bflo(hi), vn - bfhi(hi));
                LAS unsigned* x2 = (LAS unsigned*)(L + x2w);
                if ((lane & 0x31) == 0) { x2[0] = hi; x2[64] = lo; }
            }
            __syncthreads();
            const v4u of0 = *(LAS v4u*)(L + ofr), of1 = *(LAS v4u*)(L + ofr + 64);
            f32x4 a2[4];
#pragma unroll
            for (int nt = 0; nt < 4; ++nt) a2[nt] = mfma(of0, wvo[0][nt], Z4);
#pragma unroll
            for (int nt = 0; nt < 4; ++nt) a2[nt] = mfma(of1, wvo[1][nt], a2[nt]);
            if (g == 0) {
#pragma unroll
                for (int nt = 0; nt < 4; ++nt) st8(c, e3d + nt * 32768, v2u{tagf<T>(a2[nt].x + a2[nt].z), tagf<T>(a2[nt].y + a2[nt].w)});
            }
            STAMP(7 + 20 * layer);
        }
        auto ident = [&](int n, int g, int hi) {
            unsigned a[4];
#pragma unroll
            for (int q = 0; q < 4; ++q) { const int k0 = 8 * g + 2 * q - 16 * hi; a[q] = (k0 == n ? 0x3f80u : 0u) | (k0 + 1 == n ? 0x3f800000u : 0u); }
            return v4u{a[0], a[1], a[2], a[3]};
        };
        if (isRed) {
            IDS();
            nap(D_E3);
            const int ib = WS_E3 + red * 32768 + tid * 16;
            int rdw = L_RED + w * 128 + lane * 16, rdr = L_RED + (lane & 7) * 128 + (lane >> 3) * 16, e4d = WS_E4 + red * 128 + (lane >> 3) * 16;
            pini(rdw); pini(rdr); pini(e4d);
            v4u d[4];
            {
                D4x16 dd;
                poll2(c, dd, [&](D4x16& q) {
#pragma unroll
                    for (int k = 0; k < 4; ++k) q.d[k] = ld16(c, ib + k * 8192); },
                    [&](const D4x16& q) { return okbit<T>(andor<T>(andor<T>(v4<T>(q.d[0]), v4<T>(q.d[1])), andor<T>(v4<T>(q.d[2]), v4<T>(q.d[3])))); });
#pragma unroll
                for (int k = 0; k < 4; ++k) d[k] = dd.d[k];
            }
            STAMP(8 + 20 * layer);
            float a[4];
            a[0] = (u2f(d[0].x) + u2f(d[1].x)) + (u2f(d[2].x) + u2f(d[3].x));
            a[1] = (u2f(d[0].y) + u2f(d[1].y)) + (u2f(d[2].y) + u2f(d[3].y));
            a[2] = (u2f(d[0].z) + u2f(d[1].z)) + (u2f(d[2].z) + u2f(d[3].z));
            a[3] = (u2f(d[0].w) + u2f(d[1].w)) + (u2f(d[2].w) + u2f(d[3].w));
#pragma unroll
            for (int i = 0; i < 4; ++i) { a[i] += dpp<0x128>(a[i]); a[i] = x32_sum(x16_sum(a[i])); }
            if (lane < 8) *(LAS f32x4*)(L + rdw) = f32x4{a[0], a[1], a[2], a[3]};
            __syncthreads();
            if (w == 0) {
                const f32x4 v = *(LAS f32x4*)(L + rdr);
                xres += f32x4{sum8(v.x), sum8(v.y), sum8(v.z), sum8(v.w)};
                if ((lane & 7) == 0) st16(c, e4d, v4u{tagf<T>(xres.x), tagf<T>(xres.y), tagf<T>(xres.z), tagf<T>(xres.w)});
            }
            STAMP(9 + 20 * layer);
            if constexpr (layer == 0) {
                const u16* pw = p.w2 + (size_t)(224 * w + 16 * (g >> 1) + n) * MD + 16 * red + 8 * (g & 1);
                const v4u Ilo = ident(n, g, 0), Ihi = ident(n, g, 1);
                v4u raw[7];
#pragma unroll
                for (int ks = 0; ks < 7; ++ks) raw[ks] = ldnt(pw + (size_t)ks * 32 * MD);
                FENCE();
#pragma unroll
                for (int ks = 0; ks < 7; ++ks) {
                    const f32x4 a0 = mfma(raw[ks], Ilo, Z4), a1 = mfma(raw[ks], Ihi, Z4);
                    glf[ks >> 2][ks & 3] = v4u{pack2(a0.x, a0.y), pack2(a0.z, a0.w), pack2(a1.x, a1.y), pack2(a1.z, a1.w)};
                    pin(glf[ks >> 2][ks & 3]);
                }
            }
        }
        if (isGU) {
            IDS();
            if constexpr (layer == 0) {
                const v4u Ilo = ident(n, g, 0), Ihi = ident(n, g, 1);
                v4u raw[2][4];
#pragma unroll
                for (int mt = 0; mt < 4; ++mt) { raw[0][mt] = glf[0][mt]; raw[1][mt] = glf[1][mt]; }
#pragma unroll
                for (int mat = 0; mat < 2; ++mat)
#pragma unroll
                    for (int ks = 0; ks < 2; ++ks) {
                        const f32x4 l0 = mfma(raw[mat][2 * ks], Ilo, Z4), h0 = mfma(raw[mat][2 * ks], Ihi, Z4);
                        const f32x4 l1 = mfma(raw[mat][2 * ks + 1], Ilo, Z4), hh1 = mfma(raw[mat][2 * ks + 1], Ihi, Z4);
                        glf[ks][mat * 2] = v4u{pack2(l0.x, l0.y), pack2(l0.z, l0.w), pack2(l1.x, l1.y), pack2(l1.z, l1.w)};
                        glf[ks][mat * 2 + 1] = v4u{pack2(h0.x, h0.y), pack2(h0.z, h0.w), pack2(hh1.x, hh1.y), pack2(hh1.z, hh1.w)};
                        pin(glf[ks][mat * 2]); pin(glf[ks][mat * 2 + 1]);
                    }
            } else nap(D_E4);
            const int pair = 4 * (w & 3) + ((lane >> 3) & 3) + 16 * (lane >> 5);
            const GemvAddr GA = gemv_addr(w, lane, pair);
            int e5d = WS_E5 + gu * 256 + (pair & 15) * 16;
            pini(e5d);
            float x0, x1;
            {
                D2x8 dd;
                poll2(c, dd, [&](D2x8& d) { d.v = ld8(c, WS_E4 + tid * 8); d.s = ld8(c, WS_E4); },
                      [&](const D2x8& d) { return okbit<T>(andor<T>(andor<T>(d.v.x, d.v.y), andor<T>(d.s.x, d.s.y))); });
                x0 = u2f(dd.v.x) - u2f(dd.s.x); x1 = u2f(dd.v.y) - u2f(dd.s.y);
            }
            STAMP(10 + 20 * layer);
            ln_gemv_partials(x0, x1, glf, L, GA, lane);
            __syncthreads();
            if (w < 4) {
                float o[4];
                ln_gemv_final(L, GA, o);
                float hv[4];
#pragma unroll
                for (int i = 0; i < 4; ++i) {
                    auto r = __builtin_amdgcn_permlane32_swap(f2u(o[i]), f2u(o[i]), false, false);
                    const float gt = u2f(r[0]), up = u2f(r[1]);
                    hv[i] = gt * __builtin_amdgcn_rcpf(1.0f + __builtin_amdgcn_exp2f(-1.4426950408889634f * gt)) * up;
                }
                if ((lane & 0x27) == 0) st16(c, e5d, v4u{tagf<T>(hv[0]), tagf<T>(hv[1]), tagf<T>(hv[2]), tagf<T>(hv[3])});
            }
            STAMP(11 + 20 * layer);
        }
        if (isRed) {
            IDS();
            if constexpr (layer == 1) nap(D_E5);
            const int ib = WS_E5 + (112 * w + lane) * 16;
            const int ib1 = ib + (lane < 48 ? 1024 : 0);
            int hw, hr = (n < 4 ? L_A + w * 1856 + n * 464 : L_CST + 128) + 16 * g;
            { const int f = 2 * lane; hw = L_A + w * 1856 + ((f & ~31) + 8 * ((f >> 2) & 3) + 4 * ((f >> 4) & 1) + (f & 3)) * 2; }
            int rdw = L_RED + w * 128 + n * 8, rdr = L_RED + (lane & 7) * 128 + (lane >> 3) * 16, e6d = WS_E6 + red * 128 + (lane >> 3) * 16;
            pini(hw); pini(hr); pini(rdw); pini(rdr); pini(e6d);
            v4u d0, d1;
            {
                D2x16 dd;
                poll2(c, dd, [&](D2x16& d) { d.d0 = ld16(c, ib); d.d1 = ld16(c, ib1); }, [&](const D2x16& d) { return okbit<T>(andor<T>(v4<T>(d.d0), v4<T>(d.d1))); });
                d0 = dd.d0; d1 = dd.d1;
            }
            STAMP(12 + 20 * layer);
#pragma unroll
            for (int k = 0; k < 2; ++k) {
                const v4u d = k ? d1 : d0;
                const unsigned h0 = pack2(u2f(d.x), u2f(d.z)), hb1 = pack2(u2f(d.y), u2f(d.w));
                const unsigned l0 = pack2(u2f(d.x) - bflo(h0), u2f(d.z) - bfhi(h0)), lb1 = pack2(u2f(d.y) - bflo(hb1), u2f(d.w) - bfhi(hb1));
                LAS unsigned char* hs = L + hw + 256 * k;
                if (k == 0 || lane < 48) {
                    *(LAS unsigned*)hs = h0; *(LAS unsigned*)(hs + 464) = hb1;
                    *(LAS unsigned*)(hs + 928) = l0; *(LAS unsigned*)(hs + 1392) = lb1;
                }
            }
            f32x4 a = Z4, a2 = Z4;
#pragma unroll
            for (int ks = 0; ks < 7; ++ks) {
                const v4u v = *(LAS v4u*)(L + hr + 64 * ks);
                if (ks & 1) a2 = mfma(v, glf[ks >> 2][ks & 3], a2); else a = mfma(v, glf[ks >> 2][ks & 3], a);
            }
            a += a2;
            if (g == 0) *(LAS f32x2*)(L + rdw) = f32x2{a.x + a.z, a.y + a.w};
            __syncthreads();
            if (w == 0) {
                const f32x4 v = *(LAS f32x4*)(L + rdr);
                xres += f32x4{sum8(v.x), sum8(v.y), sum8(v.z), sum8(v.w)};
                if ((lane & 7) == 0) {
                    if constexpr (layer == 0) st16(c, e6d, v4u{tagf<1u>(xres.x), tagf<1u>(xres.y), tagf<1u>(xres.z), tagf<1u>(xres.w)});
                    else {
                        GAS unsigned* po = (GAS unsigned*)(p.out + 16 * red + 2 * (lane >> 3));
                        po[0] = pack2(xres.x, xres.z); po[256] = pack2(xres.y, xres.w);
                    }
                }
            }
            STAMP(13 + 20 * layer);
        }
    };
    layer_body(std::integral_constant<int, 0>{});
    layer_body(std::integral_constant<int, 1>{});
    asm volatile("mk_code_end:");
}
}

extern "C" void kernel_launch(void* const* d_in, const int* in_sizes, int n_in,
                              void* d_out, int out_size, void* d_ws, size_t ws_size,
                              hipStream_t stream) {
    mk::Params p;
    p.x = (const u16*)d_in[0]; p.ckv = (const u16*)d_in[1]; p.wq = (const u16*)d_in[2]; p.wuk = (const u16*)d_in[3];
    p.wuv = (const u16*)d_in[4]; p.wo = (const u16*)d_in[5]; p.w1 = (const u16*)d_in[7]; p.w3 = (const u16*)d_in[8];
    p.w2 = (const u16*)d_in[9]; p.out = (u16*)d_out; p.ws = (unsigned char*)d_ws;
    (void)hipMemsetAsync(d_ws, 0, mk::WS_ZERO_BYTES, stream);
    hipLaunchKernelGGL(mk::mega, dim3(mk::NWG), dim3(mk::NT), 0, stream, p);
}
```

```cpp
#include <hip/hip_runtime.h>
#include <hip/hip_bf16.h>
#include <type_traits>

typedef unsigned short u16;
typedef unsigned v4u __attribute__((ext_vector_type(4)));
typedef unsigned v2u __attribute__((ext_vector_type(2)));
typedef float f32x4 __attribute__((ext_vector_type(4)));
typedef float f32x2 __attribute__((ext_vector_type(2)));
typedef short bf16x8 __attribute__((ext_vector_type(8)));
typedef __bf16 bf16x2 __attribute__((ext_vector_type(2)));
#define GAS __attribute__((address_space(1)))
#define LAS __attribute__((address_space(3)))
#define DI __device__ __forceinline__
#define RLX_AGENT __ATOMIC_RELAXED, __HIP_MEMORY_SCOPE_AGENT

#define STAMPS 0
#define WARMPOL "nt"
#define CKV_V 4
#define GW 2
#define CKV_A 4
#define GU_EARLY 1
#define PROF 0

namespace mk {
constexpr int NWG = 256, NT = 512;
constexpr int SK = 8192, DC = 512, MD = 512, HD = 4096, FF = 1792;
constexpr int WS_CTRL = 0;
constexpr int WS_E1 = 256;
constexpr int WS_ST = WS_E1 + 65536;
constexpr int WS_E4 = WS_ST + 32768;
constexpr int WS_E5 = WS_E4 + 4096;
constexpr int WS_E6 = WS_E5 + 14336;
constexpr int WS_ZERO_BYTES = WS_E6 + 4096;
constexpr int WS_E2 = 1 << 20;
constexpr int WS_E3 = WS_E2 + 256 * 16384;
constexpr int WS_STAMP = WS_E3 + 32 * 32768;
constexpr int WS_BYTES = WS_STAMP + 256 * 64 * 8;
static_assert(WS_ZERO_BYTES <= WS_E2 && (WS_ZERO_BYTES % 16) == 0, "ws map");
constexpr int L_A = 131072;
constexpr int L_TQ = L_A + 16384;
constexpr int L_RED = L_A + 17408;
constexpr int L_P = L_RED;
constexpr int L_SMAX = L_RED + 6400;
constexpr int L_XS = L_SMAX + 512;
constexpr int L_X2 = L_XS + 4096;
constexpr int L_CST = L_X2 + 512;
constexpr int L_BYTES = 163840;
static_assert(L_CST + 640 <= L_BYTES && L_CST >= L_A + 7 * 4096, "LDS");

struct Params { const u16 *x, *ckv, *wq, *wuk, *wuv, *wo, *w1, *w3, *w2; u16* out; unsigned char* ws; };

DI float u2f(unsigned u) { return __builtin_bit_cast(float, u); }
DI unsigned f2u(float f) { return __builtin_bit_cast(unsigned, f); }
DI f32x4 mfma(v4u a, v4u b, f32x4 c) {
    return __builtin_amdgcn_mfma_f32_16x16x32_bf16(__builtin_bit_cast(bf16x8, a), __builtin_bit_cast(bf16x8, b), c, 0, 0, 0);
}
DI unsigned pack2(float a, float b) { bf16x2 v = {(__bf16)a, (__bf16)b}; return __builtin_bit_cast(unsigned, v); }
DI float bflo(unsigned u) { return u2f(u << 16); }
DI float bfhi(unsigned u) { return u2f(u & 0xffff0000u); }
template <unsigned T> DI unsigned pack2_tag(float a, float b) {
    const unsigned u = f2u(a);
    const unsigned lo = T ? ((u >> 16) | 1u) : (((u + 0x10000u) >> 17) << 1);
    return __builtin_amdgcn_perm(pack2(b, b), lo, 0x07060100u);
}
template <unsigned T> DI unsigned tagf(float a) { return T ? (f2u(a) | 1u) : (f2u(a) & ~1u); }
DI v4u ldnt(const u16* p) { return __builtin_nontemporal_load((const GAS v4u*)p); }
DI void glds16(const void* gsrc, unsigned lds_dst) {
    unsigned keep;
    asm volatile("s_mov_b32 %0, m0\n\ts_mov_b32 m0, %2\n\ts_nop 0\n\tglobal_load_lds_dwordx4 %1, off nt\n\ts_mov_b32 m0, %0" : "=&s"(keep) : "v"(gsrc), "s"(lds_dst) : "memory");
}
#define LDNT_ASM(dst, ptr) asm volatile("global_load_dwordx4 %0, %1, off nt" : "=v"(dst) : "v"(ptr) : "memory")
DI int swz16(int r) { return ((r >> 3) & 1) | ((r & 3) << 1) | ((((r >> 2) ^ (r >> 3)) & 1) << 3); }
DI int swz8(int r) { return (r & 2) | ((r >> 1) & 4); }
#define TR_ISSUE(dst, addr, OFF) asm volatile("ds_read_b64_tr_b16 %0, %1 offset:" #OFF : "=v"(dst) : "v"(addr) : "memory")

template <int CTRL> DI unsigned dppu(unsigned v) { return (unsigned)__builtin_amdgcn_update_dpp(0, (int)v, CTRL, 0xf, 0xf, true); }
template <int CTRL> DI float dpp(float v) { return u2f(dppu<CTRL>(f2u(v))); }
DI float row_sum16(float v) { v += dpp<0xB1>(v); v += dpp<0x4E>(v); v += dpp<0x141>(v); v += dpp<0x140>(v); return v; }
DI float row_max16(float v) { v = fmaxf(v, dpp<0xB1>(v)); v = fmaxf(v, dpp<0x4E>(v)); v = fmaxf(v, dpp<0x141>(v)); v = fmaxf(v, dpp<0x140>(v)); return v; }
DI float x16_sum(float v) { auto r = __builtin_amdgcn_permlane16_swap(f2u(v), f2u(v), false, false); return u2f(r[0]) + u2f(r[1]); }
DI float x32_sum(float v) { auto r = __builtin_amdgcn_permlane32_swap(f2u(v), f2u(v), false, false); return u2f(r[0]) + u2f(r[1]); }
DI float x16_max(float v) { auto r = __builtin_amdgcn_permlane16_swap(f2u(v), f2u(v), false, false); return fmaxf(u2f(r[0]), u2f(r[1])); }
DI float x32_max(float v) { auto r = __builtin_amdgcn_permlane32_swap(f2u(v), f2u(v), false, false); return fmaxf(u2f(r[0]), u2f(r[1])); }
DI float wave_sum(float v) { return x32_sum(x16_sum(row_sum16(v))); }
DI float wave_max(float v) { return x32_max(x16_max(row_max16(v))); }
DI void pin(v4u& v) { asm volatile("" : "+v"(v)); }
DI void nap(int n) {
#pragma unroll 1
    for (int i = 0; i < n; ++i) __builtin_amdgcn_s_sleep(1);
}

struct Ctx {
    __amdgpu_buffer_rsrc_t rs;
    GAS unsigned* abortw;
    unsigned long long t0;
    bool dead;
};
DI v4u ld16(const Ctx& c, int off) { return __builtin_amdgcn_raw_buffer_load_b128(c.rs, off, 0, 16); }
DI v2u ld8(const Ctx& c, int off) { return __builtin_amdgcn_raw_buffer_load_b64(c.rs, off, 0, 16); }
DI void st16(const Ctx& c, int off, v4u v) { __builtin_amdgcn_raw_buffer_store_b128(v, c.rs, off, 0, 16); }
DI void st8(const Ctx& c, int off, v2u v) { __builtin_amdgcn_raw_buffer_store_b64(v, c.rs, off, 0, 16); }
DI void st4(const Ctx& c, int off, unsigned v) { __builtin_amdgcn_raw_buffer_store_b32(v, c.rs, off, 0, 16); }
DI bool give_up(Ctx& c, int& spins) {
    if (c.dead) return true;
    if ((++spins & 63) == 0) {
        const bool late = (long long)(__builtin_amdgcn_s_memrealtime() - c.t0) > 5000000ll;
        if (late || __hip_atomic_load(c.abortw, RLX_AGENT) != 0u) { __hip_atomic_store(c.abortw, 1u, RLX_AGENT); c.dead = true; return true; }
    }
    return false;
}
template <unsigned T> DI unsigned andor(unsigned a, unsigned b) { return T ? (a & b) : (a | b); }
template <unsigned T> DI unsigned v4(v4u v) { return andor<T>(andor<T>(v.x, v.y), andor<T>(v.z, v.w)); }
template <unsigned T> DI bool okbit(unsigned m) { return (m & 1u) == T; }

DI void pini(int& v) { asm volatile("" : "+v"(v)); }
#define PP_GAP 0
template <class D, class LD, class OK> DI void poll2(Ctx& c, D& out, LD ld, OK ok) {
    int spins = 0;
    for (;;) {
        ld(out);
        if (__all(ok(out))) return;
        if (give_up(c, spins)) return;
    }
}
struct D2x8 { v2u v, s; };
struct D4x16 { v4u d[4]; };
struct D2x16 { v4u d0, d1; };
struct FragAddr { int wr, rd; };
template <bool PERM> DI FragAddr frag_addr(int w, int lane) {
    const int n = lane & 15, g = lane >> 4;
    const int pos2 = PERM ? (16 * (lane >> 5) + 4 * ((lane >> 2) & 3) + 2 * ((lane >> 4) & 1) + ((lane >> 1) & 1)) : (lane >> 1);
    FragAddr f;
    f.wr = L_XS + w * 512 + ((lane & 1) ? 256 : 0) + pos2 * 4;
    f.rd = (n < 4 ? L_XS + w * 512 + n * 128 : (n == 4 ? L_CST : L_CST + 128)) + 16 * g;
    pini(f.wr); pini(f.rd);
    return f;
}
DI void mk_frags(float v0, float v1, LAS unsigned char* L, const FragAddr& f, bool odd, v4u (&fr)[2]) {
    const unsigned hi = pack2(v0, v1);
    const unsigned lo = pack2(v0 - bflo(hi), v1 - bfhi(hi));
    const unsigned recv = dppu<0xB1>(odd ? hi : lo);
    const unsigned a = odd ? recv : hi, b = odd ? lo : recv;
    LAS unsigned* x32 = (LAS unsigned*)(L + f.wr);
    x32[0] = __builtin_amdgcn_perm(b, a, 0x05040100u); x32[32] = __builtin_amdgcn_perm(b, a, 0x07060302u);
    fr[0] = *(LAS v4u*)(L + f.rd); fr[1] = *(LAS v4u*)(L + f.rd + 64);
}
DI float sum8(float v) { v += dpp<0xB1>(v); v += dpp<0x4E>(v); v += dpp<0x141>(v); return v; }

#define IDS() int tid = threadIdx.x; asm volatile("" : "+v"(tid)); const int lane = tid & 63, n = lane & 15, g = lane >> 4; (void)n; (void)g; (void)lane
#define FENCE() __builtin_amdgcn_sched_barrier(0)
#define STAMP(i) do {} while (0)

#define D_E1A 0
#define D_E1 16
#define D_E2 40
#define D_E3 20
#define D_E4 70
#define D_E5 50
#define D_E6 130
#define D_E6R 14
#define D_E6G 50

struct GemvAddr { FragAddr f; int wq, ws, rq, rc, rs; };
DI GemvAddr gemv_addr(int w, int lane, int pair  ) {
    const int n = lane & 15, g = lane >> 4;
    GemvAddr a;
    a.f = frag_addr<true>(w, lane);
    a.wq = L_RED + w * 768 + (g == 0 ? n * 8 : 512 + n * 4);
    a.ws = L_RED + 6144 + w * 16 + (lane == 4 ? 0 : 8 + lane * 4);
    a.rq = L_RED + (lane & 7) * 768 + pair * 16;
    a.rc = L_RED + (lane & 7) * 768 + 512 + pair * 8;
    a.rs = L_RED + 6144 + (lane & 7) * 16;
    pini(a.wq); pini(a.ws); pini(a.rq); pini(a.rc); pini(a.rs);
    return a;
}
DI void ln_gemv_partials(float x0, float x1, const v4u (&bf)[2][4], LAS unsigned char* L, const GemvAddr& A, int lane) {
    const int g = lane >> 4;
    v4u xf[2];
    mk_frags(x0, x1, L, A.f, lane & 1, xf);
    const f32x4 Z = {0.f, 0.f, 0.f, 0.f};
    f32x4 a[4], gm;
#pragma unroll
    for (int ct = 0; ct < 4; ++ct) a[ct] = mfma(xf[0], bf[0][ct], Z);
    gm = mfma(xf[0], xf[0], Z);
#pragma unroll
    for (int ct = 0; ct < 4; ++ct) a[ct] = mfma(xf[1], bf[1][ct], a[ct]);
    gm = mfma(xf[1], xf[1], gm);
    if (g == 0) {
#pragma unroll
        for (int ct = 0; ct < 4; ++ct) *(LAS f32x2*)(L + A.wq + ct * 128) = f32x2{a[ct].x + a[ct].z, a[ct].y + a[ct].w};
        if (lane == 4) *(LAS f32x2*)(L + A.ws) = f32x2{gm.x + gm.z, gm.y + gm.w};
        if (lane < 2) *(LAS float*)(L + A.ws) = lane == 0 ? gm.x + 2.f * gm.z : gm.y + 2.f * gm.w;
    } else if (g == 1) {
#pragma unroll
        for (int ct = 0; ct < 4; ++ct) *(LAS float*)(L + A.wq + ct * 64) = a[ct].x;
    }
}
DI void ln_gemv_final(LAS unsigned char* L, const GemvAddr& A, float (&o)[4]) {
    f32x4 q = *(LAS f32x4*)(L + A.rq), st = *(LAS f32x4*)(L + A.rs);
    f32x2 cs = *(LAS f32x2*)(L + A.rc);
    st.x = sum8(st.x); st.y = sum8(st.y); st.z = sum8(st.z); st.w = sum8(st.w);
    q.x = sum8(q.x); q.y = sum8(q.y); q.z = sum8(q.z); q.w = sum8(q.w);
    cs.x = sum8(cs.x); cs.y = sum8(cs.y);
    const float m0 = st.x * (1.f / 512), m1 = st.y * (1.f / 512);
    const float r0 = __builtin_amdgcn_rsqf(fmaxf(st.z * (1.f / 512) - m0 * m0, 0.f) + 1e-5f);
    const float r1 = __builtin_amdgcn_rsqf(fmaxf(st.w * (1.f / 512) - m1 * m1, 0.f) + 1e-5f);
    o[0] = (q.x - m0 * cs.x) * r0; o[1] = (q.y - m1 * cs.x) * r1;
    o[2] = (q.z - m0 * cs.y) * r0; o[3] = (q.w - m1 * cs.y) * r1;
}

__global__ void __launch_bounds__(NT, 2) mega(Params p) {
    __shared__ __attribute__((aligned(16))) unsigned char smem[L_BYTES];
    LAS unsigned char* L = (LAS unsigned char*)smem;
    const int tid = threadIdx.x, lane = tid & 63, w = __builtin_amdgcn_readfirstlane(tid >> 6), n = lane & 15, g = lane >> 4;
    const int cu = blockIdx.x, xcd = cu & 7, idx = cu >> 3;
    const int h1 = xcd * 4 + (idx >> 3), j1 = idx & 7;
    const int gs = xcd * 16 + ((idx & 7) | ((idx >> 4) << 3)), bb = gs >> 6, split = gs & 63, hg = 1 - ((idx >> 3) & 1);
    const bool isRed = idx < 4, isGU = idx >= 8 && idx < 15;
    const int red = xcd * 4 + idx, gu = xcd * 7 + (idx - 8);
    unsigned cwarm = 0;
    {
        unsigned long long pc; unsigned len;
        asm volatile("mk_code_begin:\n\ts_getpc_b64 %0\n\ts_mov_b32 %1, mk_code_end-mk_code_begin" : "=s"(pc), "=s"(len));
        const unsigned long long ca = pc + (((unsigned)tid * 128u < len) ? (unsigned)tid * 128u : 0u);
        asm volatile("global_load_dword %0, %1, off" : "=v"(cwarm) : "v"(ca) : "memory");
    }
    Ctx c;
    c.rs = __builtin_amdgcn_make_buffer_rsrc((void*)p.ws, 0, WS_BYTES, 0x00020000);
    c.abortw = (GAS unsigned*)(p.ws + WS_CTRL);
    c.t0 = __builtin_amdgcn_s_memrealtime();
    c.dead = false;
    STAMP(0);
    unsigned xcc_; asm volatile("s_getreg_b32 %0, hwreg(HW_REG_XCC_ID)" : "=s"(xcc_));
    const unsigned kk_ = (((xcc_ & 15) - (unsigned)cu) & 7u) + 1u;
    if (cu == 0 && tid == 0) st4(c, 64, kk_);
    const f32x4 Z4 = {0.f, 0.f, 0.f, 0.f};

    v4u ck[16];
    v4u ckT[4][4];
    v4u glf[2][4];
    const u16* ckv0 = p.ckv + ((size_t)bb * SK + (size_t)split * 128) * DC;
    unsigned xr0, xr1, xr2, xr3, xs0, xs1;
    {
        const u16* px = p.x + tid;
        const u16* pr = p.x + (16 * (red & 31) + 2 * (lane >> 3));
        asm volatile("global_load_ushort %0, %6, off\n\tglobal_load_ushort %1, %6, off offset:1024\n\tglobal_load_dword %2, %7, off\n\tglobal_load_dword %3, %7, off offset:1024\n\t"
                     "global_load_ushort %4, %8, off\n\tglobal_load_ushort %5, %8, off offset:1024"
                     : "=&v"(xr0), "=&v"(xr1), "=&v"(xr2), "=&v"(xr3), "=&v"(xs0), "=&v"(xs1) : "v"(px), "v"(pr), "v"(p.x) : "memory");
    }

    v4u wqk[2][4];
    const unsigned lds0 = (unsigned)(size_t)smem;
    auto ckv_rows = [&](int i0 = 0, int i1 = 16) {
#pragma unroll 4
        for (int i = i0; i < i1; ++i) glds16(ckv0 + (size_t)(16 * w + i) * DC + ((lane ^ swz16(i)) * 8), lds0 + (16 * w + i) * 1024);
    };
    {
        v4u aq[4][4];
#pragma unroll
        for (int i = 0; i < 2; ++i) {
            const int r = 8 * w + 4 * i + g;
            glds16(p.wuk + (size_t)(64 * j1 + r) * HD + h1 * 128 + ((n ^ swz16(r & 15)) * 8), lds0 + L_A + (8 * w + 4 * i) * 256);
        }
        {
            const u16* pq = p.wq + (size_t)(64 * w + g) * HD + h1 * 128 + 8 * n;
#pragma unroll
            for (int mt = 0; mt < 4; ++mt)
#pragma unroll
                for (int i = 0; i < 4; ++i) { const u16* pp = pq + (size_t)(16 * mt + 4 * i) * HD; LDNT_ASM(aq[mt][i], pp); }
        }
        asm volatile("s_waitcnt vmcnt(16)" : "+v"(xr0), "+v"(xr1), "+v"(xr2), "+v"(xr3), "+v"(xs0), "+v"(xs1), "+v"(cwarm) :: "memory");
        STAMP(14);
        __syncthreads();
        STAMP(15);
        const int fs = swz16(n);
        v4u bu[4][4];
#pragma unroll
        for (int ct = 0; ct < 4; ++ct)
#pragma unroll
            for (int s = 0; s < 4; ++s) bu[ct][s] = *(LAS v4u*)(L + L_A + (16 * ct + n) * 256 + (((4 * s + g) ^ fs) * 16));
#pragma unroll
        for (int ct = 0; ct < 4; ++ct)
#pragma unroll
            for (int s = 0; s < 4; ++s) pin(bu[ct][s]);
        __syncthreads();
        STAMP(16);
        if (w != 7) ckv_rows(0, CKV_A);
        else {
            const v4u z = {0u, 0u, 0u, 0u};
            const int ib = WS_E2 + (h1 * 8 + j1) * 16384 + lane * 16;
#pragma unroll
            for (int i = 0; i < 16; ++i) st16(c, ib + i * 1024, z);
#pragma unroll
            for (int i = 0; i < 4; ++i) st16(c, WS_E3 + cu * 4096 + lane * 16 + i * 1024, z);
        }
        STAMP(17);
#define WAITQ(NA, NB, mt) asm volatile("s_cmp_lt_u32 %4, 7\n\ts_cbranch_scc1 1f\n\ts_waitcnt vmcnt(" #NB ")\n\ts_branch 2f\n1:\n\ts_waitcnt vmcnt(%5)\n2:" \
            : "+v"(aq[mt][0]), "+v"(aq[mt][1]), "+v"(aq[mt][2]), "+v"(aq[mt][3]) : "s"(w), "n"(NA + CKV_A) : "memory", "scc")
        LAS unsigned char* tq = L + L_A + w * 4096;
#pragma unroll
        for (int mt = 0; mt < 4; ++mt) {
            if (mt == 0) WAITQ(12, 32, 0);
            else if (mt == 1) WAITQ(8, 28, 1);
            else if (mt == 2) WAITQ(4, 24, 2);
            else { WAITQ(0, 0, 3); STAMP(18); }
#pragma unroll
            for (int i = 0; i < 4; ++i) { const int r = 4 * i + g; *(LAS v4u*)(tq + r * 256 + ((n ^ swz16(r)) * 16)) = aq[mt][i]; }
            v4u af[4];
#pragma unroll
            for (int s = 0; s < 4; ++s) af[s] = *(LAS v4u*)(tq + n * 256 + (((4 * s + g) ^ fs) * 16));
#pragma unroll
            for (int ct = 0; ct < 4; ++ct) {
                f32x4 a0 = Z4;
#pragma unroll
                for (int s = 0; s < 4; ++s) a0 = mfma(af[s], bu[ct][s], a0);
                if (mt & 1) { wqk[mt >> 1][ct].z = pack2(a0.x, a0.y); wqk[mt >> 1][ct].w = pack2(a0.z, a0.w); }
                else { wqk[mt >> 1][ct].x = pack2(a0.x, a0.y); wqk[mt >> 1][ct].y = pack2(a0.z, a0.w); }
            }
        }
        if (w != 7) ckv_rows(CKV_A, 16); else ckv_rows();
#pragma unroll
        for (int ks = 0; ks < 2; ++ks)
#pragma unroll
            for (int ct = 0; ct < 4; ++ct) pin(wqk[ks][ct]);
    }
    if (w == 7 && lane < 40) *(LAS v4u*)(L + L_CST + lane * 16) = lane < 8 ? v4u{0x3f803f80u, 0x3f803f80u, 0x3f803f80u, 0x3f803f80u} : v4u{0u, 0u, 0u, 0u};
    __syncthreads();
    STAMP(1);
    f32x4 xres = {bflo(xr2), bflo(xr3), bfhi(xr2), bfhi(xr3)};

    auto wo_rows = [&](int v) {
        const int rr = lane >> 3;
#pragma unroll 4
        for (int i = 0; i < 16; ++i) {
            const int d = 8 * i + rr;
            glds16(p.wo + (size_t)(h1 * 128 + d) * MD + 64 * v + (((lane & 7) ^ swz8(d)) * 8), lds0 + v * 16384 + i * 1024);
        }
    };
    unsigned warm = 0;
    auto layer_body = [&](auto LC) {
        constexpr int layer = decltype(LC)::value;
        constexpr unsigned T = layer == 0 ? 1u : 0u;
        {
            IDS();
            const int pair = (tid >> 3) & 31;
            const GemvAddr GA = gemv_addr(w, lane, pair);
            int dst = WS_E1 + ((h1 & 1) * 16 + (h1 >> 1)) * 1024 + (64 * j1 + 2 * pair) * 2;
            pini(dst);
            float x0, x1;
            if constexpr (layer == 0) { x0 = bflo(xr0) - bflo(xs0); x1 = bflo(xr1) - bflo(xs1); }
            else {
#pragma unroll
                for (int ks = 0; ks < 2; ++ks)
#pragma unroll
                    for (int ct = 0; ct < 4; ++ct) wqk[ks][ct] = *(LAS v4u*)(L + w * 16384 + 8192 + (ks * 4 + ct) * 1024 + lane * 16);
                nap(isRed ? D_E6R : (isGU ? D_E6G : D_E6));
                D2x8 dd;
                poll2(c, dd, [&](D2x8& d) { d.v = ld8(c, WS_E6 + tid * 8); d.s = ld8(c, WS_E6); }, [&](const D2x8& d) { return okbit<1u>(d.v.x & d.v.y & d.s.x & d.s.y); });
                x0 = u2f(dd.v.x) - u2f(dd.s.x); x1 = u2f(dd.v.y) - u2f(dd.s.y);
                STAMP(20);
            }
            ln_gemv_partials(x0, x1, wqk, L, GA, lane);
            __syncthreads();
            if (w < 4) {
                float o[4];
                ln_gemv_final(L, GA, o);
                const float SC = 0.08838834764831845f * 1.4426950408889634f;
                if ((lane & 7) == 0) {
                    st4(c, dst, pack2_tag<T>(o[0] * SC, o[2] * SC));
                    st4(c, dst + 32768, pack2_tag<T>(o[1] * SC, o[3] * SC));
                }
            }
            STAMP(2 + 20 * layer);
        }
        v4u wuvr[2];
        {
            IDS();
            if constexpr (layer == 0) {
                {
                    const u16* pu = p.wuv + (size_t)(64 * j1 + (tid >> 4)) * HD + h1 * 128 + (tid & 15) * 8;
                    const u16* pu2 = pu + (size_t)32 * HD;
                    LDNT_ASM(wuvr[0], pu); LDNT_ASM(wuvr[1], pu2);
                }
                asm volatile("s_waitcnt vmcnt(2)" ::: "memory");
                STAMP(19);
                __syncthreads();
                const int fs = swz16(n);
#pragma unroll
                for (int s = 0; s < 16; ++s) ck[s] = *(LAS v4u*)(L + (16 * w + n) * 1024 + (((4 * s + g) ^ fs) * 16));
                v2u tr[4][8];
                const int q = n >> 2, pp = n & 3;
#pragma unroll
                for (int t = 0; t < 2; ++t) {
                    const int r = 8 * g + 4 * t + q;
#pragma unroll
                    for (int ct = 0; ct < 4; ++ct) {
                        const unsigned ad = lds0 + r * 1024 + (((8 * w + 2 * ct + (pp >> 1)) ^ swz16(r & 15)) * 16) + 8 * (pp & 1);
                        TR_ISSUE(tr[ct][0 + t], ad, 0); TR_ISSUE(tr[ct][2 + t], ad, 32768);
                        const unsigned ad2 = ad + 65536;
                        TR_ISSUE(tr[ct][4 + t], ad2, 0); TR_ISSUE(tr[ct][6 + t], ad2, 32768);
                    }
                }
                asm volatile("s_waitcnt lgkmcnt(0)" : "+v"(tr[0][0]), "+v"(tr[0][1]), "+v"(tr[0][2]), "+v"(tr[0][3]), "+v"(tr[0][4]), "+v"(tr[0][5]), "+v"(tr[0][6]), "+v"(tr[0][7]),
                             "+v"(tr[1][0]), "+v"(tr[1][1]), "+v"(tr[1][2]), "+v"(tr[1][3]), "+v"(tr[1][4]), "+v"(tr[1][5]), "+v"(tr[1][6]), "+v"(tr[1][7]) :: "memory");
                asm volatile("" : "+v"(tr[2][0]), "+v"(tr[2][1]), "+v"(tr[2][2]), "+v"(tr[2][3]), "+v"(tr[2][4]), "+v"(tr[2][5]), "+v"(tr[2][6]), "+v"(tr[2][7]),
                             "+v"(tr[3][0]), "+v"(tr[3][1]), "+v"(tr[3][2]), "+v"(tr[3][3]), "+v"(tr[3][4]), "+v"(tr[3][5]), "+v"(tr[3][6]), "+v"(tr[3][7]) :: "memory");
#pragma unroll
                for (int ct = 0; ct < 4; ++ct)
#pragma unroll
                    for (int s = 0; s < 4; ++s) ckT[ct][s] = v4u{tr[ct][2 * s].x, tr[ct][2 * s].y, tr[ct][2 * s + 1].x, tr[ct][2 * s + 1].y};
#pragma unroll
                for (int s = 0; s < 16; ++s) pin(ck[s]);
                __syncthreads();
                STAMP(17);
                if (w >= 4) wo_rows(w - 4);
            }
            if (w < 4) {
                const int t = tid;
                v4u qg[4];
                const int e1 = WS_E1 + (bb * 2 + hg) * 16384 + t * 16;
                nap(layer == 0 ? D_E1A : D_E1);
                D4x16 dd;
                poll2(c, dd, [&](D4x16& d) {
#pragma unroll
                    for (int k = 0; k < 4; ++k) d.d[k] = ld16(c, e1 + k * 4096); },
                    [&](const D4x16& d) { return okbit<T>(andor<T>(andor<T>(v4<T>(d.d[0]), v4<T>(d.d[1])), andor<T>(v4<T>(d.d[2]), v4<T>(d.d[3])))); });
#pragma unroll
                for (int k = 0; k < 4; ++k) qg[k] = dd.d[k];
#pragma unroll
                for (int k = 0; k < 4; ++k) { const int u = t + 256 * k; *(LAS v4u*)(L + L_A + (u >> 6) * 1040 + (u & 63) * 16) = qg[k]; }
            }
        }
        {
            IDS();
            int e2a = WS_E2 + ((2 * (n & 7) + hg) * 8 + w) * 16384 + (bb * 64 + split) * 128 + 16 * g + (n < 8 ? 0 : 64);
            int e2b = WS_E2 + ((2 * ((n & 7) + 8) + hg) * 8 + w) * 16384 + (bb * 64 + split) * 128 + 16 * g + (n < 8 ? 64 : 0);
            int qab = L_A + n * 1040 + 16 * g, smw = L_SMAX + (n * 8 + w) * 4, smr = L_SMAX + n * 32;
            int pw_ = L_P + n * 272 + (16 * w + 4 * g) * 2, pr_ = L_P + n * 272 + 16 * g;
            pini(qab); pini(e2a); pini(e2b); pini(smw); pini(smr); pini(pw_); pini(pr_);
            __syncthreads();
            STAMP(3 + 20 * layer);
            f32x4 s0 = Z4, s1 = Z4;
            {
                v4u q[8];
#pragma unroll
                for (int s = 0; s < 8; ++s) q[s] = *(LAS v4u*)(L + qab + 64 * s);
                FENCE();
#pragma unroll
                for (int s = 0; s < 8; s += 2) {
                    s0 = mfma(ck[s], q[s], s0); s1 = mfma(ck[s + 1], q[s + 1], s1);
                    q[s] = *(LAS v4u*)(L + qab + 64 * (s + 8)); q[s + 1] = *(LAS v4u*)(L + qab + 64 * (s + 9));
                }
#pragma unroll
                for (int s = 0; s < 8; s += 2) { s0 = mfma(ck[s + 8], q[s], s0); s1 = mfma(ck[s + 9], q[s + 1], s1); }
            }
            s0 += s1;
            float mx = fmaxf(fmaxf(s0.x, s0.y), fmaxf(s0.z, s0.w));
            mx = x32_max(x16_max(mx));
            if (g == 0) *(LAS float*)(L + smw) = mx;
            __syncthreads();
            if constexpr (layer == 0) {
                asm volatile("s_cmp_lt_u32 %2, 4\n\ts_cbranch_scc1 1f\n\ts_waitcnt vmcnt(16)\n\ts_branch 2f\n1:\n\ts_waitcnt vmcnt(0)\n2:" : "+v"(wuvr[0]), "+v"(wuvr[1]) : "s"(w) : "memory", "scc");
                const int r = tid >> 4;
                LAS unsigned char* dst = L + L_A + r * 256 + (((tid & 15) ^ swz16(r & 15)) * 16);
                *(LAS v4u*)dst = wuvr[0]; *(LAS v4u*)(dst + 32 * 256) = wuvr[1];
            }
            const f32x4 ma = *(LAS f32x4*)(L + smr), mb = *(LAS f32x4*)(L + smr + 16);
            const float M = fmaxf(fmaxf(fmaxf(ma.x, ma.y), fmaxf(ma.z, ma.w)), fmaxf(fmaxf(mb.x, mb.y), fmaxf(mb.z, mb.w)));
            const unsigned p01 = pack2(__builtin_amdgcn_exp2f(s0.x - M), __builtin_amdgcn_exp2f(s0.y - M));
            const unsigned p23 = pack2(__builtin_amdgcn_exp2f(s0.z - M), __builtin_amdgcn_exp2f(s0.w - M));
            *(LAS v2u*)(L + pw_) = v2u{p01, p23};
            if constexpr (layer == 0) asm volatile("s_waitcnt vmcnt(0)" ::: "memory");
            __syncthreads();
            v4u pf[4];
#pragma unroll
            for (int s = 0; s < 4; ++s) pf[s] = *(LAS v4u*)(L + pr_ + 64 * s);
            const int h = 2 * n + hg;
            v4u ov[2];
#pragma unroll
            for (int q = 0; q < 2; ++q) {
                f32x4 a = Z4, b = Z4;
#pragma unroll
                for (int s = 0; s < 4; ++s) { a = mfma(ckT[2 * q][s], pf[s], a); b = mfma(ckT[2 * q + 1][s], pf[s], b); }
                ov[q] = v4u{pack2_tag<T>(a.x, a.y), pack2_tag<T>(a.z, a.w), pack2_tag<T>(b.x, b.y), pack2_tag<T>(b.z, b.w)};
            }
            {
                const v4u r1 = {dppu<0x128>(ov[1].x), dppu<0x128>(ov[1].y), dppu<0x128>(ov[1].z), dppu<0x128>(ov[1].w)};
                const bool lo8 = n < 8;
                st16(c, e2a, lo8 ? ov[0] : r1);
                st16(c, e2b, lo8 ? r1 : ov[0]);
            }
            if (w == 0) {
                const v4u ones = {0x3f803f80u, 0x3f803f80u, 0x3f803f80u, 0x3f803f80u};
                f32x4 l = Z4;
#pragma unroll
                for (int s = 0; s < 4; ++s) l = mfma(ones, pf[s], l);
                if (g == 0) st8(c, WS_ST + ((bb * 32 + h) * 64 + split) * 8, v2u{tagf<T>(M), tagf<T>(l.x)});
            }
            STAMP(4 + 20 * layer);
            if constexpr (layer == 0) {
                if (w >= 4) wo_rows(w);
            }
        }
        v4u wvo[2][4];
        if constexpr (layer == 0) {
            IDS();
            if (w >= 4) asm volatile("s_waitcnt vmcnt(0)" ::: "memory");
            v2u tr[4][8];
            const int q = n >> 2, pp = n & 3;
#pragma unroll
            for (int t = 0; t < 2; ++t) {
                const int d = 8 * g + 4 * t + q;
#pragma unroll
                for (int nt = 0; nt < 4; ++nt) {
                    const unsigned ad = lds0 + w * 16384 + d * 128 + (((2 * nt + (pp >> 1)) ^ swz8(d)) * 16) + 8 * (pp & 1);
                    TR_ISSUE(tr[nt][0 + t], ad, 0); TR_ISSUE(tr[nt][2 + t], ad, 4096); TR_ISSUE(tr[nt][4 + t], ad, 8192); TR_ISSUE(tr[nt][6 + t], ad, 12288);
                }
            }
            asm volatile("s_waitcnt lgkmcnt(0)" : "+v"(tr[0][0]), "+v"(tr[0][1]), "+v"(tr[0][2]), "+v"(tr[0][3]), "+v"(tr[0][4]), "+v"(tr[0][5]), "+v"(tr[0][6]), "+v"(tr[0][7]),
                         "+v"(tr[1][0]), "+v"(tr[1][1]), "+v"(tr[1][2]), "+v"(tr[1][3]), "+v"(tr[1][4]), "+v"(tr[1][5]), "+v"(tr[1][6]), "+v"(tr[1][7]) :: "memory");
            asm volatile("" : "+v"(tr[2][0]), "+v"(tr[2][1]), "+v"(tr[2][2]), "+v"(tr[2][3]), "+v"(tr[2][4]), "+v"(tr[2][5]), "+v"(tr[2][6]), "+v"(tr[2][7]),
                         "+v"(tr[3][0]), "+v"(tr[3][1]), "+v"(tr[3][2]), "+v"(tr[3][3]), "+v"(tr[3][4]), "+v"(tr[3][5]), "+v"(tr[3][6]), "+v"(tr[3][7]) :: "memory");
#pragma unroll
            for (int ks = 0; ks < 2; ++ks)
#pragma unroll
                for (int ct = 0; ct < 4; ++ct) *(LAS v4u*)(L + w * 16384 + 8192 + (ks * 4 + ct) * 1024 + lane * 16) = wqk[ks][ct];
            const int fs = swz16(n);
#pragma unroll
            for (int ks = 0; ks < 2; ++ks) {
                v4u u0[4], u1[4];
#pragma unroll
                for (int s = 0; s < 4; ++s) {
                    u0[s] = *(LAS v4u*)(L + L_A + (32 * ks + n) * 256 + (((4 * s + g) ^ fs) * 16));
                    u1[s] = *(LAS v4u*)(L + L_A + (32 * ks + 16 + n) * 256 + (((4 * s + g) ^ fs) * 16));
                }
#pragma unroll
                for (int nt = 0; nt < 4; ++nt) {
                    f32x4 a0 = Z4, a1 = Z4;
#pragma unroll
                    for (int s = 0; s < 4; ++s) {
                        const v4u bo = v4u{tr[nt][2 * s].x, tr[nt][2 * s].y, tr[nt][2 * s + 1].x, tr[nt][2 * s + 1].y};
                        a0 = mfma(u0[s], bo, a0); a1 = mfma(u1[s], bo, a1);
                    }
                    *(LAS v4u*)(L + w * 16384 + (ks * 4 + nt) * 1024 + lane * 16) =
                        v4u{pack2(a0.x, a0.y), pack2(a0.z, a0.w), pack2(a1.x, a1.y), pack2(a1.z, a1.w)};
                }
                FENCE();
            }
            if (w < 4) {
                    const u16* pb = tid < 56 ? p.w2 + (size_t)(56 * idx + tid) * MD + 64 * xcd
                                             : ((tid < 136 ? p.w1 : p.w3) + (size_t)(16 * idx + ((tid - 56) % 80) / 5) * FF + min(224 * xcd + 64 * (((tid - 56) % 80) % 5), FF - 2));
                    asm volatile("global_load_dword %0, %1, off" : "=v"(warm) : "v"(pb) : "memory");
            }
            __syncthreads();
        }
        STAMP(5 + 20 * layer);
        {
            IDS();
#pragma unroll
            for (int ks = 0; ks < 2; ++ks)
#pragma unroll
                for (int nt = 0; nt < 4; ++nt) wvo[ks][nt] = *(LAS v4u*)(L + w * 16384 + (ks * 4 + nt) * 1024 + lane * 16);
            if constexpr (layer == 1) nap(D_E2);
            const int b = w >> 2, chunk = lane & 7, sp0 = (tid >> 3) & 31;
            const int ib = WS_E2 + (h1 * 8 + j1) * 16384 + (b * 64 + sp0) * 128 + chunk * 16;
            const int sb = WS_ST + ((b * 32 + h1) * 64 + lane) * 8;
            const FragAddr FA = frag_addr<false>(w, lane);
            int imo = L_A + (b * 64 + sp0) * 128 + ((chunk ^ swz8(sp0)) * 16);
            int tra = (int)lds0 + L_A + (b * 64 + 8 * g + (n >> 2)) * 128 + (((2 * (w & 3) + ((n & 3) >> 1)) ^ swz8(8 * g + (n >> 2))) * 16) + 8 * (n & 1);
            int x2w = L_X2 + (b * 32 + 8 * (w & 3) + (n >> 1)) * 4;
            int ofr = (n < 4 ? L_X2 + n * 128 : L_CST + 128) + 16 * g;
            int e3d = WS_E3 + 4 * w * 32768 + cu * 128 + n * 8;
            pini(imo); pini(tra); pini(x2w); pini(ofr); pini(e3d);
            v4u d0, d1; v2u ls;
            int spins = 0;
            for (;;) {
                d0 = ld16(c, ib); d1 = ld16(c, ib + 32 * 128); ls = ld8(c, sb);
                if (__all(okbit<T>(andor<T>(andor<T>(v4<T>(d0), v4<T>(d1)), andor<T>(ls.x, ls.y))))) break;
                if (give_up(c, spins)) break;
            }
            STAMP(6 + 20 * layer);
            if constexpr (layer == 0) {
                asm volatile("" ::"v"(warm));
                const int gq = isGU ? gu : 0;
#pragma unroll
                for (int mt = 0; mt < 4; ++mt) {
                    const u16* pa = p.w1 + (size_t)(64 * w + 16 * mt + n) * FF + 32 * gq + 8 * g;
                    const u16* pb = p.w3 + (size_t)(64 * w + 16 * mt + n) * FF + 32 * gq + 8 * g;
                    if (isGU) { glf[0][mt] = ldnt(pa); glf[1][mt] = ldnt(pb); }
                }
                FENCE();
            }
            *(LAS v4u*)(L + imo) = d0; *(LAS v4u*)(L + imo + 32 * 128) = d1;
            const float Mi = u2f(ls.x);
            const float e = __builtin_amdgcn_exp2f(Mi - wave_max(Mi));
            const float wn = e * __builtin_amdgcn_rcpf(wave_sum(e * u2f(ls.y)));
            v4u wf[2];
            mk_frags(wn, 0.f, L, FA, lane & 1, wf);
            __syncthreads();
            {
                v2u t0, t1, t2, t3;
                asm volatile("ds_read_b64_tr_b16 %0, %4\n\tds_read_b64_tr_b16 %1, %4 offset:512\n\tds_read_b64_tr_b16 %2, %4 offset:4096\n\tds_read_b64_tr_b16 %3, %4 offset:4608\n\ts_waitcnt lgkmcnt(0)"
                             : "=&v"(t0), "=&v"(t1), "=&v"(t2), "=&v"(t3) : "v"(tra) : "memory");
                f32x4 o = mfma(wf[0], v4u{t0.x, t0.y, t1.x, t1.y}, Z4);
                o = mfma(wf[1], v4u{t2.x, t2.y, t3.x, t3.y}, o);
                const float v = o.x + o.z;
                const float vn = dpp<0xB1>(v);
                const unsigned hi = pack2(v, vn);
                const unsigned lo = pack2(v - bflo(hi), vn - bfhi(hi));
                LAS unsigned* x2 = (LAS unsigned*)(L + x2w);
                if ((lane & 0x31) == 0) { x2[0] = hi; x2[64] = lo; }
            }
            __syncthreads();
            const v4u of0 = *(LAS v4u*)(L + ofr), of1 = *(LAS v4u*)(L + ofr + 64);
            f32x4 a2[4];
#pragma unroll
            for (int nt = 0; nt < 4; ++nt) a2[nt] = mfma(of0, wvo[0][nt], Z4);
#pragma unroll
            for (int nt = 0; nt < 4; ++nt) a2[nt] = mfma(of1, wvo[1][nt], a2[nt]);
            if (g == 0) {
#pragma unroll
                for (int nt = 0; nt < 4; ++nt) st8(c, e3d + nt * 32768, v2u{tagf<T>(a2[nt].x + a2[nt].z), tagf<T>(a2[nt].y + a2[nt].w)});
            }
            STAMP(7 + 20 * layer);
        }
        auto ident = [&](int n, int g, int hi) {
            unsigned a[4];
#pragma unroll
            for (int q = 0; q < 4; ++q) { const int k0 = 8 * g + 2 * q - 16 * hi; a[q] = (k0 == n ? 0x3f80u : 0u) | (k0 + 1 == n ? 0x3f800000u : 0u); }
            return v4u{a[0], a[1], a[2], a[3]};
        };
        if (isRed) {
            IDS();
            nap(D_E3);
            const int ib = WS_E3 + red * 32768 + tid * 16;
            int rdw = L_RED + w * 128 + lane * 16, rdr = L_RED + (lane & 7) * 128 + (lane >> 3) * 16, e4d = WS_E4 + red * 128 + (lane >> 3) * 16;
            pini(rdw); pini(rdr); pini(e4d);
            v4u d[4];
            {
                D4x16 dd;
                poll2(c, dd, [&](D4x16& q) {
#pragma unroll
                    for (int k = 0; k < 4; ++k) q.d[k] = ld16(c, ib + k * 8192); },
                    [&](const D4x16& q) { return okbit<T>(andor<T>(andor<T>(v4<T>(q.d[0]), v4<T>(q.d[1])), andor<T>(v4<T>(q.d[2]), v4<T>(q.d[3])))); });
#pragma unroll
                for (int k = 0; k < 4; ++k) d[k] = dd.d[k];
            }
            STAMP(8 + 20 * layer);
            float a[4];
            a[0] = (u2f(d[0].x) + u2f(d[1].x)) + (u2f(d[2].x) + u2f(d[3].x));
            a[1] = (u2f(d[0].y) + u2f(d[1].y)) + (u2f(d[2].y) + u2f(d[3].y));
            a[2] = (u2f(d[0].z) + u2f(d[1].z)) + (u2f(d[2].z) + u2f(d[3].z));
            a[3] = (u2f(d[0].w) + u2f(d[1].w)) + (u2f(d[2].w) + u2f(d[3].w));
#pragma unroll
            for (int i = 0; i < 4; ++i) { a[i] += dpp<0x128>(a[i]); a[i] = x32_sum(x16_sum(a[i])); }
            if (lane < 8) *(LAS f32x4*)(L + rdw) = f32x4{a[0], a[1], a[2], a[3]};
            __syncthreads();
            if (w == 0) {
                const f32x4 v = *(LAS f32x4*)(L + rdr);
                xres += f32x4{sum8(v.x), sum8(v.y), sum8(v.z), sum8(v.w)};
                if ((lane & 7) == 0) st16(c, e4d, v4u{tagf<T>(xres.x), tagf<T>(xres.y), tagf<T>(xres.z), tagf<T>(xres.w)});
            }
            STAMP(9 + 20 * layer);
            if constexpr (layer == 0) {
                const u16* pw = p.w2 + (size_t)(224 * w + 16 * (g >> 1) + n) * MD + 16 * red + 8 * (g & 1);
                const v4u Ilo = ident(n, g, 0), Ihi = ident(n, g, 1);
                v4u raw[7];
#pragma unroll
                for (int ks = 0; ks < 7; ++ks) raw[ks] = ldnt(pw + (size_t)ks * 32 * MD);
                FENCE();
#pragma unroll
                for (int ks = 0; ks < 7; ++ks) {
                    const f32x4 a0 = mfma(raw[ks], Ilo, Z4), a1 = mfma(raw[ks], Ihi, Z4);
                    glf[ks >> 2][ks & 3] = v4u{pack2(a0.x, a0.y), pack2(a0.z, a0.w), pack2(a1.x, a1.y), pack2(a1.z, a1.w)};
                    pin(glf[ks >> 2][ks & 3]);
                }
            }
        }
        if (isGU) {
            IDS();
            if constexpr (layer == 0) {
                const v4u Ilo = ident(n, g, 0), Ihi = ident(n, g, 1);
                v4u raw[2][4];
#pragma unroll
                for (int mt = 0; mt < 4; ++mt) { raw[0][mt] = glf[0][mt]; raw[1][mt] = glf[1][mt]; }
#pragma unroll
                for (int mat = 0; mat < 2; ++mat)
#pragma unroll
                    for (int ks = 0; ks < 2; ++ks) {
                        const f32x4 l0 = mfma(raw[mat][2 * ks], Ilo, Z4), h0 = mfma(raw[mat][2 * ks], Ihi, Z4);
                        const f32x4 l1 = mfma(raw[mat][2 * ks + 1], Ilo, Z4), hh1 = mfma(raw[mat][2 * ks + 1], Ihi, Z4);
                        glf[ks][mat * 2] = v4u{pack2(l0.x, l0.y), pack2(l0.z, l0.w), pack2(l1.x, l1.y), pack2(l1.z, l1.w)};
                        glf[ks][mat * 2 + 1] = v4u{pack2(h0.x, h0.y), pack2(h0.z, h0.w), pack2(hh1.x, hh1.y), pack2(hh1.z, hh1.w)};
                        pin(glf[ks][mat * 2]); pin(glf[ks][mat * 2 + 1]);
                    }
            } else nap(D_E4);
            const int pair = 4 * (w & 3) + ((lane >> 3) & 3) + 16 * (lane >> 5);
            const GemvAddr GA = gemv_addr(w, lane, pair);
            int e5d = WS_E5 + gu * 256 + (pair & 15) * 16;
            pini(e5d);
            float x0, x1;
            {
                D2x8 dd;
                poll2(c, dd, [&](D2x8& d) { d.v = ld8(c, WS_E4 + tid * 8); d.s = ld8(c, WS_E4); },
                      [&](const D2x8& d) { return okbit<T>(andor<T>(andor<T>(d.v.x, d.v.y), andor<T>(d.s.x, d.s.y))); });
                x0 = u2f(dd.v.x) - u2f(dd.s.x); x1 = u2f(dd.v.y) - u2f(dd.s.y);
            }
            STAMP(10 + 20 * layer);
            ln_gemv_partials(x0, x1, glf, L, GA, lane);
            __syncthreads();
            if (w < 4) {
                float o[4];
                ln_gemv_final(L, GA, o);
                float hv[4];
#pragma unroll
                for (int i = 0; i < 4; ++i) {
                    auto r = __builtin_amdgcn_permlane32_swap(f2u(o[i]), f2u(o[i]), false, false);
                    const float gt = u2f(r[0]), up = u2f(r[1]);
                    hv[i] = gt * __builtin_amdgcn_rcpf(1.0f + __builtin_amdgcn_exp2f(-1.4426950408889634f * gt)) * up;
                }
                if ((lane & 0x27) == 0) st16(c, e5d, v4u{tagf<T>(hv[0]), tagf<T>(hv[1]), tagf<T>(hv[2]), tagf<T>(hv[3])});
            }
            STAMP(11 + 20 * layer);
        }
        if (isRed) {
            IDS();
            if constexpr (layer == 1) nap(D_E5);
            const int ib = WS_E5 + (112 * w + lane) * 16;
            const int ib1 = ib + (lane < 48 ? 1024 : 0);
            int hw, hr = (n < 4 ? L_A + w * 1856 + n * 464 : L_CST + 128) + 16 * g;
            { const int f = 2 * lane; hw = L_A + w * 1856 + ((f & ~31) + 8 * ((f >> 2) & 3) + 4 * ((f >> 4) & 1) + (f & 3)) * 2; }
            int rdw = L_RED + w * 128 + n * 8, rdr = L_RED + (lane & 7) * 128 + (lane >> 3) * 16, e6d = WS_E6 + red * 128 + (lane >> 3) * 16;
            pini(hw); pini(hr); pini(rdw); pini(rdr); pini(e6d);
            v4u d0, d1;
            {
                D2x16 dd;
                poll2(c, dd, [&](D2x16& d) { d.d0 = ld16(c, ib); d.d1 = ld16(c, ib1); }, [&](const D2x16& d) { return okbit<T>(andor<T>(v4<T>(d.d0), v4<T>(d.d1))); });
                d0 = dd.d0; d1 = dd.d1;
            }
            STAMP(12 + 20 * layer);
#pragma unroll
            for (int k = 0; k < 2; ++k) {
                const v4u d = k ? d1 : d0;
                const unsigned h0 = pack2(u2f(d.x), u2f(d.z)), hb1 = pack2(u2f(d.y), u2f(d.w));
                const unsigned l0 = pack2(u2f(d.x) - bflo(h0), u2f(d.z) - bfhi(h0)), lb1 = pack2(u2f(d.y) - bflo(hb1), u2f(d.w) - bfhi(hb1));
                LAS unsigned char* hs = L + hw + 256 * k;
                if (k == 0 || lane < 48) {
                    *(LAS unsigned*)hs = h0; *(LAS unsigned*)(hs + 464) = hb1;
                    *(LAS unsigned*)(hs + 928) = l0; *(LAS unsigned*)(hs + 1392) = lb1;
                }
            }
            f32x4 a = Z4, a2 = Z4;
#pragma unroll
            for (int ks = 0; ks < 7; ++ks) {
                const v4u v = *(LAS v4u*)(L + hr + 64 * ks);
                if (ks & 1) a2 = mfma(v, glf[ks >> 2][ks & 3], a2); else a = mfma(v, glf[ks >> 2][ks & 3], a);
            }
            a += a2;
            if (g == 0) *(LAS f32x2*)(L + rdw) = f32x2{a.x + a.z, a.y + a.w};
            __syncthreads();
            if (w == 0) {
                const f32x4 v = *(LAS f32x4*)(L + rdr);
                xres += f32x4{sum8(v.x), sum8(v.y), sum8(v.z), sum8(v.w)};
                if ((lane & 7) == 0) {
                    if constexpr (layer == 0) st16(c, e6d, v4u{tagf<1u>(xres.x), tagf<1u>(xres.y), tagf<1u>(xres.z), tagf<1u>(xres.w)});
                    else {
                        GAS unsigned* po = (GAS unsigned*)(p.out + 16 * red + 2 * (lane >> 3));
                        po[0] = pack2(xres.x, xres.z); po[256] = pack2(xres.y, xres.w);
                    }
                }
            }
            STAMP(13 + 20 * layer);
        }
    };
    layer_body(std::integral_constant<int, 0>{});
    layer_body(std::integral_constant<int, 1>{});
    {
        const unsigned k0 = __builtin_amdgcn_raw_buffer_load_b32(c.rs, 64, 0, 16);
        if (k0 != kk_) { for (int i = 0; i < 4000; ++i) __builtin_amdgcn_s_sleep(1); }
    }
    asm volatile("mk_code_end:");
}
}

extern "C" void kernel_launch(void* const* d_in, const int* in_sizes, int n_in,
                              void* d_out, int out_size, void* d_ws, size_t ws_size,
                              hipStream_t stream) {
    mk::Params p;
    p.x = (const u16*)d_in[0]; p.ckv = (const u16*)d_in[1]; p.wq = (const u16*)d_in[2]; p.wuk = (const u16*)d_in[3];
    p.wuv = (const u16*)d_in[4]; p.wo = (const u16*)d_in[5]; p.w1 = (const u16*)d_in[7]; p.w3 = (const u16*)d_in[8];
    p.w2 = (const u16*)d_in[9]; p.out = (u16*)d_out; p.ws = (unsigned char*)d_ws;
    (void)hipMemsetAsync(d_ws, 0, mk::WS_ZERO_BYTES, stream);
    hipLaunchKernelGGL(mk::mega, dim3(mk::NWG), dim3(mk::NT), 0, stream, p);
}
```

```cpp
#include <hip/hip_runtime.h>
#include <hip/hip_bf16.h>
#include <type_traits>

typedef unsigned short u16;
typedef unsigned v4u __attribute__((ext_vector_type(4)));
typedef unsigned v2u __attribute__((ext_vector_type(2)));
typedef float f32x4 __attribute__((ext_vector_type(4)));
typedef float f32x2 __attribute__((ext_vector_type(2)));
typedef short bf16x8 __attribute__((ext_vector_type(8)));
typedef __bf16 bf16x2 __attribute__((ext_vector_type(2)));
#define GAS __attribute__((address_space(1)))
#define LAS __attribute__((address_space(3)))
#define DI __device__ __forceinline__
#define RLX_AGENT __ATOMIC_RELAXED, __HIP_MEMORY_SCOPE_AGENT

#define STAMPS 0
#define WARMPOL "nt"
#define CKV_V 4
#define GW 2
#define CKV_A 4
#define GU_EARLY 1
#define PROF 0

namespace mk {
constexpr int NWG = 256, NT = 512;
constexpr int SK = 8192, DC = 512, MD = 512, HD = 4096, FF = 1792;
constexpr int WS_CTRL = 0;
constexpr int WS_E1 = 256;
constexpr int WS_ST = WS_E1 + 65536;
constexpr int WS_E4 = WS_ST + 32768;
constexpr int WS_E5 = WS_E4 + 4096;
constexpr int WS_E6 = WS_E5 + 14336;
constexpr int WS_ZERO_BYTES = WS_E6 + 4096;
constexpr int WS_E2 = 1 << 20;
constexpr int WS_E3 = WS_E2 + 256 * 16384;
constexpr int WS_STAMP = WS_E3 + 32 * 32768;
constexpr int WS_BYTES = WS_STAMP + 256 * 64 * 8;
static_assert(WS_ZERO_BYTES <= WS_E2 && (WS_ZERO_BYTES % 16) == 0, "ws map");
constexpr int L_A = 131072;
constexpr int L_TQ = L_A + 16384;
constexpr int L_RED = L_A + 17408;
constexpr int L_P = L_RED;
constexpr int L_SMAX = L_RED + 6400;
constexpr int L_XS = L_SMAX + 512;
constexpr int L_X2 = L_XS + 4096;
constexpr int L_CST = L_X2 + 512;
constexpr int L_BYTES = 163840;
static_assert(L_CST + 640 <= L_BYTES && L_CST >= L_A + 7 * 4096, "LDS");

struct Params { const u16 *x, *ckv, *wq, *wuk, *wuv, *wo, *w1, *w3, *w2; u16* out; unsigned char* ws; };

DI float u2f(unsigned u) { return __builtin_bit_cast(float, u); }
DI unsigned f2u(float f) { return __builtin_bit_cast(unsigned, f); }
DI f32x4 mfma(v4u a, v4u b, f32x4 c) {
    return __builtin_amdgcn_mfma_f32_16x16x32_bf16(__builtin_bit_cast(bf16x8, a), __builtin_bit_cast(bf16x8, b), c, 0, 0, 0);
}
DI unsigned pack2(float a, float b) { bf16x2 v = {(__bf16)a, (__bf16)b}; return __builtin_bit_cast(unsigned, v); }
DI float bflo(unsigned u) { return u2f(u << 16); }
DI float bfhi(unsigned u) { return u2f(u & 0xffff0000u); }
template <unsigned T> DI unsigned pack2_tag(float a, float b) {
    const unsigned u = f2u(a);
    const unsigned lo = T ? ((u >> 16) | 1u) : (((u + 0x10000u) >> 17) << 1);
    return __builtin_amdgcn_perm(pack2(b, b), lo, 0x07060100u);
}
template <unsigned T> DI unsigned tagf(float a) { return T ? (f2u(a) | 1u) : (f2u(a) & ~1u); }
DI v4u ldnt(const u16* p) { return __builtin_nontemporal_load((const GAS v4u*)p); }
DI void glds16(const void* gsrc, unsigned lds_dst) {
    unsigned keep;
    asm volatile("s_mov_b32 %0, m0\n\ts_mov_b32 m0, %2\n\ts_nop 0\n\tglobal_load_lds_dwordx4 %1, off nt\n\ts_mov_b32 m0, %0" : "=&s"(keep) : "v"(gsrc), "s"(lds_dst) : "memory");
}
#define LDNT_ASM(dst, ptr) asm volatile("global_load_dwordx4 %0, %1, off nt" : "=v"(dst) : "v"(ptr) : "memory")
DI int swz16(int r) { return ((r >> 3) & 1) | ((r & 3) << 1) | ((((r >> 2) ^ (r >> 3)) & 1) << 3); }
DI int swz8(int r) { return (r & 2) | ((r >> 1) & 4); }
#define TR_ISSUE(dst, addr, OFF) asm volatile("ds_read_b64_tr_b16 %0, %1 offset:" #OFF : "=v"(dst) : "v"(addr) : "memory")

template <int CTRL> DI unsigned dppu(unsigned v) { return (unsigned)__builtin_amdgcn_update_dpp(0, (int)v, CTRL, 0xf, 0xf, true); }
template <int CTRL> DI float dpp(float v) { return u2f(dppu<CTRL>(f2u(v))); }
DI float row_sum16(float v) { v += dpp<0xB1>(v); v += dpp<0x4E>(v); v += dpp<0x141>(v); v += dpp<0x140>(v); return v; }
DI float row_max16(float v) { v = fmaxf(v, dpp<0xB1>(v)); v = fmaxf(v, dpp<0x4E>(v)); v = fmaxf(v, dpp<0x141>(v)); v = fmaxf(v, dpp<0x140>(v)); return v; }
DI float x16_sum(float v) { auto r = __builtin_amdgcn_permlane16_swap(f2u(v), f2u(v), false, false); return u2f(r[0]) + u2f(r[1]); }
DI float x32_sum(float v) { auto r = __builtin_amdgcn_permlane32_swap(f2u(v), f2u(v), false, false); return u2f(r[0]) + u2f(r[1]); }
DI float x16_max(float v) { auto r = __builtin_amdgcn_permlane16_swap(f2u(v), f2u(v), false, false); return fmaxf(u2f(r[0]), u2f(r[1])); }
DI float x32_max(float v) { auto r = __builtin_amdgcn_permlane32_swap(f2u(v), f2u(v), false, false); return fmaxf(u2f(r[0]), u2f(r[1])); }
DI float wave_sum(float v) { return x32_sum(x16_sum(row_sum16(v))); }
DI float wave_max(float v) { return x32_max(x16_max(row_max16(v))); }
DI void pin(v4u& v) { asm volatile("" : "+v"(v)); }
DI void nap(int n) {
#pragma unroll 1
    for (int i = 0; i < n; ++i) __builtin_amdgcn_s_sleep(1);
}

struct Ctx {
    __amdgpu_buffer_rsrc_t rs;
    GAS unsigned* abortw;
    unsigned long long t0;
    bool dead;
};
DI v4u ld16(const Ctx& c, int off) { return __builtin_amdgcn_raw_buffer_load_b128(c.rs, off, 0, 16); }
DI v2u ld8(const Ctx& c, int off) { return __builtin_amdgcn_raw_buffer_load_b64(c.rs, off, 0, 16); }
DI void st16(const Ctx& c, int off, v4u v) { __builtin_amdgcn_raw_buffer_store_b128(v, c.rs, off, 0, 16); }
DI void st8(const Ctx& c, int off, v2u v) { __builtin_amdgcn_raw_buffer_store_b64(v, c.rs, off, 0, 16); }
DI void st4(const Ctx& c, int off, unsigned v) { __builtin_amdgcn_raw_buffer_store_b32(v, c.rs, off, 0, 16); }
DI bool give_up(Ctx& c, int& spins) {
    if (c.dead) return true;
    if ((++spins & 63) == 0) {
        const bool late = (long long)(__builtin_amdgcn_s_memrealtime() - c.t0) > 5000000ll;
        if (late || __hip_atomic_load(c.abortw, RLX_AGENT) != 0u) { __hip_atomic_store(c.abortw, 1u, RLX_AGENT); c.dead = true; return true; }
    }
    return false;
}
template <unsigned T> DI unsigned andor(unsigned a, unsigned b) { return T ? (a & b) : (a | b); }
template <unsigned T> DI unsigned v4(v4u v) { return andor<T>(andor<T>(v.x, v.y), andor<T>(v.z, v.w)); }
template <unsigned T> DI bool okbit(unsigned m) { return (m & 1u) == T; }

DI void pini(int& v) { asm volatile("" : "+v"(v)); }
#define PP_GAP 0
template <class D, class LD, class OK> DI void poll2(Ctx& c, D& out, LD ld, OK ok) {
    int spins = 0;
    for (;;) {
        ld(out);
        if (__all(ok(out))) return;
        if (give_up(c, spins)) return;
    }
}
struct D2x8 { v2u v, s; };
struct D4x16 { v4u d[4]; };
struct D2x16 { v4u d0, d1; };
struct FragAddr { int wr, rd; };
template <bool PERM> DI FragAddr frag_addr(int w, int lane) {
    const int n = lane & 15, g = lane >> 4;
    const int pos2 = PERM ? (16 * (lane >> 5) + 4 * ((lane >> 2) & 3) + 2 * ((lane >> 4) & 1) + ((lane >> 1) & 1)) : (lane >> 1);
    FragAddr f;
    f.wr = L_XS + w * 512 + ((lane & 1) ? 256 : 0) + pos2 * 4;
    f.rd = (n < 4 ? L_XS + w * 512 + n * 128 : (n == 4 ? L_CST : L_CST + 128)) + 16 * g;
    pini(f.wr); pini(f.rd);
    return f;
}
DI void mk_frags(float v0, float v1, LAS unsigned char* L, const FragAddr& f, bool odd, v4u (&fr)[2]) {
    const unsigned hi = pack2(v0, v1);
    const unsigned lo = pack2(v0 - bflo(hi), v1 - bfhi(hi));
    const unsigned recv = dppu<0xB1>(odd ? hi : lo);
    const unsigned a = odd ? recv : hi, b = odd ? lo : recv;
    LAS unsigned* x32 = (LAS unsigned*)(L + f.wr);
    x32[0] = __builtin_amdgcn_perm(b, a, 0x05040100u); x32[32] = __builtin_amdgcn_perm(b, a, 0x07060302u);
    fr[0] = *(LAS v4u*)(L + f.rd); fr[1] = *(LAS v4u*)(L + f.rd + 64);
}
DI float sum8(float v) { v += dpp<0xB1>(v); v += dpp<0x4E>(v); v += dpp<0x141>(v); return v; }

#define IDS() int tid = threadIdx.x; asm volatile("" : "+v"(tid)); const int lane = tid & 63, n = lane & 15, g = lane >> 4; (void)n; (void)g; (void)lane
#define FENCE() __builtin_amdgcn_sched_barrier(0)
#define STAMP(i) do {} while (0)

#define D_E1A 0
#define D_E1 16
#define D_E2 40
#define D_E3 20
#define D_E4 70
#define D_E5 50
#define D_E6 130
#define D_E6R 14
#define D_E6G 50

struct GemvAddr { FragAddr f; int wq, ws, rq, rc, rs; };
DI GemvAddr gemv_addr(int w, int lane, int pair  ) {
    const int n = lane & 15, g = lane >> 4;
    GemvAddr a;
    a.f = frag_addr<true>(w, lane);
    a.wq = L_RED + w * 768 + (g == 0 ? n * 8 : 512 + n * 4);
    a.ws = L_RED + 6144 + w * 16 + (lane == 4 ? 0 : 8 + lane * 4);
    a.rq = L_RED + (lane & 7) * 768 + pair * 16;
    a.rc = L_RED + (lane & 7) * 768 + 512 + pair * 8;
    a.rs = L_RED + 6144 + (lane & 7) * 16;
    pini(a.wq); pini(a.ws); pini(a.rq); pini(a.rc); pini(a.rs);
    return a;
}
DI void ln_gemv_partials(float x0, float x1, const v4u (&bf)[2][4], LAS unsigned char* L, const GemvAddr& A, int lane) {
    const int g = lane >> 4;
    v4u xf[2];
    mk_frags(x0, x1, L, A.f, lane & 1, xf);
    const f32x4 Z = {0.f, 0.f, 0.f, 0.f};
    f32x4 a[4], gm;
#pragma unroll
    for (int ct = 0; ct < 4; ++ct) a[ct] = mfma(xf[0], bf[0][ct], Z);
    gm = mfma(xf[0], xf[0], Z);
#pragma unroll
    for (int ct = 0; ct < 4; ++ct) a[ct] = mfma(xf[1], bf[1][ct], a[ct]);
    gm = mfma(xf[1], xf[1], gm);
    if (g == 0) {
#pragma unroll
        for (int ct = 0; ct < 4; ++ct) *(LAS f32x2*)(L + A.wq + ct * 128) = f32x2{a[ct].x + a[ct].z, a[ct].y + a[ct].w};
        if (lane == 4) *(LAS f32x2*)(L + A.ws) = f32x2{gm.x + gm.z, gm.y + gm.w};
        if (lane < 2) *(LAS float*)(L + A.ws) = lane == 0 ? gm.x + 2.f * gm.z : gm.y + 2.f * gm.w;
    } else if (g == 1) {
#pragma unroll
        for (int ct = 0; ct < 4; ++ct) *(LAS float*)(L + A.wq + ct * 64) = a[ct].x;
    }
}
DI void ln_gemv_final(LAS unsigned char* L, const GemvAddr& A, float (&o)[4]) {
    f32x4 q = *(LAS f32x4*)(L + A.rq), st = *(LAS f32x4*)(L + A.rs);
    f32x2 cs = *(LAS f32x2*)(L + A.rc);
    st.x = sum8(st.x); st.y = sum8(st.y); st.z = sum8(st.z); st.w = sum8(st.w);
    q.x = sum8(q.x); q.y = sum8(q.y); q.z = sum8(q.z); q.w = sum8(q.w);
    cs.x = sum8(cs.x); cs.y = sum8(cs.y);
    const float m0 = st.x * (1.f / 512), m1 = st.y * (1.f / 512);
    const float r0 = __builtin_amdgcn_rsqf(fmaxf(st.z * (1.f / 512) - m0 * m0, 0.f) + 1e-5f);
    const float r1 = __builtin_amdgcn_rsqf(fmaxf(st.w * (1.f / 512) - m1 * m1, 0.f) + 1e-5f);
    o[0] = (q.x - m0 * cs.x) * r0; o[1] = (q.y - m1 * cs.x) * r1;
    o[2] = (q.z - m0 * cs.y) * r0; o[3] = (q.w - m1 * cs.y) * r1;
}

__global__ void __launch_bounds__(NT, 2) mega(Params p) {
    __shared__ __attribute__((aligned(16))) unsigned char smem[L_BYTES];
    LAS unsigned char* L = (LAS unsigned char*)smem;
    const int tid = threadIdx.x, lane = tid & 63, w = __builtin_amdgcn_readfirstlane(tid >> 6), n = lane & 15, g = lane >> 4;
    const int cu = blockIdx.x, xcd = cu & 7, idx = cu >> 3;
    const int h1 = xcd * 4 + (idx >> 3), j1 = idx & 7;
    const int gs = xcd * 16 + ((idx & 7) | ((idx >> 4) << 3)), bb = gs >> 6, split = gs & 63, hg = 1 - ((idx >> 3) & 1);
    const bool isRed = idx < 4, isGU = idx >= 8 && idx < 15;
    const int red = xcd * 4 + idx, gu = xcd * 7 + (idx - 8);
    unsigned cwarm = 0;
    {
        unsigned long long pc; unsigned len;
        asm volatile("mk_code_begin:\n\ts_getpc_b64 %0\n\ts_mov_b32 %1, mk_code_end-mk_code_begin" : "=s"(pc), "=s"(len));
        const unsigned long long ca = pc + (((unsigned)tid * 128u < len) ? (unsigned)tid * 128u : 0u);
        asm volatile("global_load_dword %0, %1, off" : "=v"(cwarm) : "v"(ca) : "memory");
    }
    Ctx c;
    c.rs = __builtin_amdgcn_make_buffer_rsrc((void*)p.ws, 0, WS_BYTES, 0x00020000);
    c.abortw = (GAS unsigned*)(p.ws + WS_CTRL);
    c.t0 = __builtin_amdgcn_s_memrealtime();
    c.dead = false;
    STAMP(0);
    const f32x4 Z4 = {0.f, 0.f, 0.f, 0.f};

    v4u ck[16];
    v4u ckT[4][4];
    v4u glf[2][4];
    const u16* ckv0 = p.ckv + ((size_t)bb * SK + (size_t)split * 128) * DC;
    unsigned xr0, xr1, xr2, xr3, xs0, xs1;
    {
        const u16* px = p.x + tid;
        const u16* pr = p.x + (16 * (red & 31) + 2 * (lane >> 3));
        asm volatile("global_load_ushort %0, %6, off\n\tglobal_load_ushort %1, %6, off offset:1024\n\tglobal_load_dword %2, %7, off\n\tglobal_load_dword %3, %7, off offset:1024\n\t"
                     "global_load_ushort %4, %8, off\n\tglobal_load_ushort %5, %8, off offset:1024"
                     : "=&v"(xr0), "=&v"(xr1), "=&v"(xr2), "=&v"(xr3), "=&v"(xs0), "=&v"(xs1) : "v"(px), "v"(pr), "v"(p.x) : "memory");
    }

    v4u wqk[2][4];
    const unsigned lds0 = (unsigned)(size_t)smem;
    auto ckv_rows = [&](int i0 = 0, int i1 = 16) {
#pragma unroll 4
        for (int i = i0; i < i1; ++i) glds16(ckv0 + (size_t)(16 * w + i) * DC + ((lane ^ swz16(i)) * 8), lds0 + (16 * w + i) * 1024);
    };
    {
        v4u aq[4][4];
#pragma unroll
        for (int i = 0; i < 2; ++i) {
            const int r = 8 * w + 4 * i + g;
            glds16(p.wuk + (size_t)(64 * j1 + r) * HD + h1 * 128 + ((n ^ swz16(r & 15)) * 8), lds0 + L_A + (8 * w + 4 * i) * 256);
        }
        {
            const u16* pq = p.wq + (size_t)(64 * w + g) * HD + h1 * 128 + 8 * n;
#pragma unroll
            for (int mt = 0; mt < 4; ++mt)
#pragma unroll
                for (int i = 0; i < 4; ++i) { const u16* pp = pq + (size_t)(16 * mt + 4 * i) * HD; LDNT_ASM(aq[mt][i], pp); }
        }
        asm volatile("s_waitcnt vmcnt(16)" : "+v"(xr0), "+v"(xr1), "+v"(xr2), "+v"(xr3), "+v"(xs0), "+v"(xs1), "+v"(cwarm) :: "memory");
        STAMP(14);
        __syncthreads();
        STAMP(15);
        const int fs = swz16(n);
        v4u bu[4][4];
#pragma unroll
        for (int ct = 0; ct < 4; ++ct)
#pragma unroll
            for (int s = 0; s < 4; ++s) bu[ct][s] = *(LAS v4u*)(L + L_A + (16 * ct + n) * 256 + (((4 * s + g) ^ fs) * 16));
#pragma unroll
        for (int ct = 0; ct < 4; ++ct)
#pragma unroll
            for (int s = 0; s < 4; ++s) pin(bu[ct][s]);
        __syncthreads();
        STAMP(16);
        if (w != 7) ckv_rows(0, CKV_A);
        else {
            const v4u z = {0u, 0u, 0u, 0u};
            const int ib = WS_E2 + (h1 * 8 + j1) * 16384 + lane * 16;
#pragma unroll
            for (int i = 0; i < 16; ++i) st16(c, ib + i * 1024, z);
#pragma unroll
            for (int i = 0; i < 4; ++i) st16(c, WS_E3 + cu * 4096 + lane * 16 + i * 1024, z);
        }
        STAMP(17);
#define WAITQ(NA, NB, mt) asm volatile("s_cmp_lt_u32 %4, 7\n\ts_cbranch_scc1 1f\n\ts_waitcnt vmcnt(" #NB ")\n\ts_branch 2f\n1:\n\ts_waitcnt vmcnt(%5)\n2:" \
            : "+v"(aq[mt][0]), "+v"(aq[mt][1]), "+v"(aq[mt][2]), "+v"(aq[mt][3]) : "s"(w), "n"(NA + CKV_A) : "memory", "scc")
        LAS unsigned char* tq = L + L_A + w * 4096;
#pragma unroll
        for (int mt = 0; mt < 4; ++mt) {
            if (mt == 0) WAITQ(12, 32, 0);
            else if (mt == 1) WAITQ(8, 28, 1);
            else if (mt == 2) WAITQ(4, 24, 2);
            else { WAITQ(0, 0, 3); STAMP(18); }
#pragma unroll
            for (int i = 0; i < 4; ++i) { const int r = 4 * i + g; *(LAS v4u*)(tq + r * 256 + ((n ^ swz16(r)) * 16)) = aq[mt][i]; }
            v4u af[4];
#pragma unroll
            for (int s = 0; s < 4; ++s) af[s] = *(LAS v4u*)(tq + n * 256 + (((4 * s + g) ^ fs) * 16));
#pragma unroll
            for (int ct = 0; ct < 4; ++ct) {
                f32x4 a0 = Z4;
#pragma unroll
                for (int s = 0; s < 4; ++s) a0 = mfma(af[s], bu[ct][s], a0);
                if (mt & 1) { wqk[mt >> 1][ct].z = pack2(a0.x, a0.y); wqk[mt >> 1][ct].w = pack2(a0.z, a0.w); }
                else { wqk[mt >> 1][ct].x = pack2(a0.x, a0.y); wqk[mt >> 1][ct].y = pack2(a0.z, a0.w); }
            }
        }
        if (w != 7) ckv_rows(CKV_A, 16); else ckv_rows();
#pragma unroll
        for (int ks = 0; ks < 2; ++ks)
#pragma unroll
            for (int ct = 0; ct < 4; ++ct) pin(wqk[ks][ct]);
    }
    if (w == 7 && lane < 40) *(LAS v4u*)(L + L_CST + lane * 16) = lane < 8 ? v4u{0x3f803f80u, 0x3f803f80u, 0x3f803f80u, 0x3f803f80u} : v4u{0u, 0u, 0u, 0u};
    __syncthreads();
    STAMP(1);
    f32x4 xres = {bflo(xr2), bflo(xr3), bfhi(xr2), bfhi(xr3)};

    auto wo_rows = [&](int v) {
        const int rr = lane >> 3;
#pragma unroll 4
        for (int i = 0; i < 16; ++i) {
            const int d = 8 * i + rr;
            glds16(p.wo + (size_t)(h1 * 128 + d) * MD + 64 * v + (((lane & 7) ^ swz8(d)) * 8), lds0 + v * 16384 + i * 1024);
        }
    };
    unsigned warm = 0;
    unsigned long long tq_ = 0;
    auto layer_body = [&](auto LC) {
        constexpr int layer = decltype(LC)::value;
        constexpr unsigned T = layer == 0 ? 1u : 0u;
        {
            IDS();
            const int pair = (tid >> 3) & 31;
            const GemvAddr GA = gemv_addr(w, lane, pair);
            int dst = WS_E1 + ((h1 & 1) * 16 + (h1 >> 1)) * 1024 + (64 * j1 + 2 * pair) * 2;
            pini(dst);
            float x0, x1;
            if constexpr (layer == 0) { x0 = bflo(xr0) - bflo(xs0); x1 = bflo(xr1) - bflo(xs1); }
            else {
#pragma unroll
                for (int ks = 0; ks < 2; ++ks)
#pragma unroll
                    for (int ct = 0; ct < 4; ++ct) wqk[ks][ct] = *(LAS v4u*)(L + w * 16384 + 8192 + (ks * 4 + ct) * 1024 + lane * 16);
                nap(isRed ? D_E6R : (isGU ? D_E6G : D_E6));
                D2x8 dd;
                poll2(c, dd, [&](D2x8& d) { d.v = ld8(c, WS_E6 + tid * 8); d.s = ld8(c, WS_E6); }, [&](const D2x8& d) { return okbit<1u>(d.v.x & d.v.y & d.s.x & d.s.y); });
                x0 = u2f(dd.v.x) - u2f(dd.s.x); x1 = u2f(dd.v.y) - u2f(dd.s.y);
                STAMP(20);
            }
            ln_gemv_partials(x0, x1, wqk, L, GA, lane);
            __syncthreads();
            if (w < 4) {
                float o[4];
                ln_gemv_final(L, GA, o);
                const float SC = 0.08838834764831845f * 1.4426950408889634f;
                if ((lane & 7) == 0) {
                    st4(c, dst, pack2_tag<T>(o[0] * SC, o[2] * SC));
                    st4(c, dst + 32768, pack2_tag<T>(o[1] * SC, o[3] * SC));
                }
            }
            STAMP(2 + 20 * layer);
        }
        v4u wuvr[2];
        {
            IDS();
            if constexpr (layer == 0) {
                {
                    const u16* pu = p.wuv + (size_t)(64 * j1 + (tid >> 4)) * HD + h1 * 128 + (tid & 15) * 8;
                    const u16* pu2 = pu + (size_t)32 * HD;
                    LDNT_ASM(wuvr[0], pu); LDNT_ASM(wuvr[1], pu2);
                }
                asm volatile("s_waitcnt vmcnt(2)" ::: "memory");
                STAMP(19);
                __syncthreads();
                const int fs = swz16(n);
#pragma unroll
                for (int s = 0; s < 16; ++s) ck[s] = *(LAS v4u*)(L + (16 * w + n) * 1024 + (((4 * s + g) ^ fs) * 16));
                v2u tr[4][8];
                const int q = n >> 2, pp = n & 3;
#pragma unroll
                for (int t = 0; t < 2; ++t) {
                    const int r = 8 * g + 4 * t + q;
#pragma unroll
                    for (int ct = 0; ct < 4; ++ct) {
                        const unsigned ad = lds0 + r * 1024 + (((8 * w + 2 * ct + (pp >> 1)) ^ swz16(r & 15)) * 16) + 8 * (pp & 1);
                        TR_ISSUE(tr[ct][0 + t], ad, 0); TR_ISSUE(tr[ct][2 + t], ad, 32768);
                        const unsigned ad2 = ad + 65536;
                        TR_ISSUE(tr[ct][4 + t], ad2, 0); TR_ISSUE(tr[ct][6 + t], ad2, 32768);
                    }
                }
                asm volatile("s_waitcnt lgkmcnt(0)" : "+v"(tr[0][0]), "+v"(tr[0][1]), "+v"(tr[0][2]), "+v"(tr[0][3]), "+v"(tr[0][4]), "+v"(tr[0][5]), "+v"(tr[0][6]), "+v"(tr[0][7]),
                             "+v"(tr[1][0]), "+v"(tr[1][1]), "+v"(tr[1][2]), "+v"(tr[1][3]), "+v"(tr[1][4]), "+v"(tr[1][5]), "+v"(tr[1][6]), "+v"(tr[1][7]) :: "memory");
                asm volatile("" : "+v"(tr[2][0]), "+v"(tr[2][1]), "+v"(tr[2][2]), "+v"(tr[2][3]), "+v"(tr[2][4]), "+v"(tr[2][5]), "+v"(tr[2][6]), "+v"(tr[2][7]),
                             "+v"(tr[3][0]), "+v"(tr[3][1]), "+v"(tr[3][2]), "+v"(tr[3][3]), "+v"(tr[3][4]), "+v"(tr[3][5]), "+v"(tr[3][6]), "+v"(tr[3][7]) :: "memory");
#pragma unroll
                for (int ct = 0; ct < 4; ++ct)
#pragma unroll
                    for (int s = 0; s < 4; ++s) ckT[ct][s] = v4u{tr[ct][2 * s].x, tr[ct][2 * s].y, tr[ct][2 * s + 1].x, tr[ct][2 * s + 1].y};
#pragma unroll
                for (int s = 0; s < 16; ++s) pin(ck[s]);
                __syncthreads();
                STAMP(17);
                if (w >= 4) wo_rows(w - 4);
            }
            if (w < 4) {
                const int t = tid;
                v4u qg[4];
                const int e1 = WS_E1 + (bb * 2 + hg) * 16384 + t * 16;
                nap(layer == 0 ? D_E1A : D_E1);
                D4x16 dd;
                poll2(c, dd, [&](D4x16& d) {
#pragma unroll
                    for (int k = 0; k < 4; ++k) d.d[k] = ld16(c, e1 + k * 4096); },
                    [&](const D4x16& d) { return okbit<T>(andor<T>(andor<T>(v4<T>(d.d[0]), v4<T>(d.d[1])), andor<T>(v4<T>(d.d[2]), v4<T>(d.d[3])))); });
#pragma unroll
                for (int k = 0; k < 4; ++k) qg[k] = dd.d[k];
#pragma unroll
                for (int k = 0; k < 4; ++k) { const int u = t + 256 * k; *(LAS v4u*)(L + L_A + (u >> 6) * 1040 + (u & 63) * 16) = qg[k]; }
            }
        }
        {
            IDS();
            int e2a = WS_E2 + ((2 * (n & 7) + hg) * 8 + w) * 16384 + (bb * 64 + split) * 128 + 16 * g + (n < 8 ? 0 : 64);
            int e2b = WS_E2 + ((2 * ((n & 7) + 8) + hg) * 8 + w) * 16384 + (bb * 64 + split) * 128 + 16 * g + (n < 8 ? 64 : 0);
            int qab = L_A + n * 1040 + 16 * g, smw = L_SMAX + (n * 8 + w) * 4, smr = L_SMAX + n * 32;
            int pw_ = L_P + n * 272 + (16 * w + 4 * g) * 2, pr_ = L_P + n * 272 + 16 * g;
            pini(qab); pini(e2a); pini(e2b); pini(smw); pini(smr); pini(pw_); pini(pr_);
            __syncthreads();
            if constexpr (layer == 0) tq_ = __builtin_amdgcn_s_memrealtime() - c.t0;
            STAMP(3 + 20 * layer);
            f32x4 s0 = Z4, s1 = Z4;
            {
                v4u q[8];
#pragma unroll
                for (int s = 0; s < 8; ++s) q[s] = *(LAS v4u*)(L + qab + 64 * s);
                FENCE();
#pragma unroll
                for (int s = 0; s < 8; s += 2) {
                    s0 = mfma(ck[s], q[s], s0); s1 = mfma(ck[s + 1], q[s + 1], s1);
                    q[s] = *(LAS v4u*)(L + qab + 64 * (s + 8)); q[s + 1] = *(LAS v4u*)(L + qab + 64 * (s + 9));
                }
#pragma unroll
                for (int s = 0; s < 8; s += 2) { s0 = mfma(ck[s + 8], q[s], s0); s1 = mfma(ck[s + 9], q[s + 1], s1); }
            }
            s0 += s1;
            float mx = fmaxf(fmaxf(s0.x, s0.y), fmaxf(s0.z, s0.w));
            mx = x32_max(x16_max(mx));
            if (g == 0) *(LAS float*)(L + smw) = mx;
            __syncthreads();
            if constexpr (layer == 0) {
                asm volatile("s_cmp_lt_u32 %2, 4\n\ts_cbranch_scc1 1f\n\ts_waitcnt vmcnt(16)\n\ts_branch 2f\n1:\n\ts_waitcnt vmcnt(0)\n2:" : "+v"(wuvr[0]), "+v"(wuvr[1]) : "s"(w) : "memory", "scc");
                const int r = tid >> 4;
                LAS unsigned char* dst = L + L_A + r * 256 + (((tid & 15) ^ swz16(r & 15)) * 16);
                *(LAS v4u*)dst = wuvr[0]; *(LAS v4u*)(dst + 32 * 256) = wuvr[1];
            }
            const f32x4 ma = *(LAS f32x4*)(L + smr), mb = *(LAS f32x4*)(L + smr + 16);
            const float M = fmaxf(fmaxf(fmaxf(ma.x, ma.y), fmaxf(ma.z, ma.w)), fmaxf(fmaxf(mb.x, mb.y), fmaxf(mb.z, mb.w)));
            const unsigned p01 = pack2(__builtin_amdgcn_exp2f(s0.x - M), __builtin_amdgcn_exp2f(s0.y - M));
            const unsigned p23 = pack2(__builtin_amdgcn_exp2f(s0.z - M), __builtin_amdgcn_exp2f(s0.w - M));
            *(LAS v2u*)(L + pw_) = v2u{p01, p23};
            if constexpr (layer == 0) asm volatile("s_waitcnt vmcnt(0)" ::: "memory");
            __syncthreads();
            v4u pf[4];
#pragma unroll
            for (int s = 0; s < 4; ++s) pf[s] = *(LAS v4u*)(L + pr_ + 64 * s);
            const int h = 2 * n + hg;
            v4u ov[2];
#pragma unroll
            for (int q = 0; q < 2; ++q) {
                f32x4 a = Z4, b = Z4;
#pragma unroll
                for (int s = 0; s < 4; ++s) { a = mfma(ckT[2 * q][s], pf[s], a); b = mfma(ckT[2 * q + 1][s], pf[s], b); }
                ov[q] = v4u{pack2_tag<T>(a.x, a.y), pack2_tag<T>(a.z, a.w), pack2_tag<T>(b.x, b.y), pack2_tag<T>(b.z, b.w)};
            }
            {
                const v4u r1 = {dppu<0x128>(ov[1].x), dppu<0x128>(ov[1].y), dppu<0x128>(ov[1].z), dppu<0x128>(ov[1].w)};
                const bool lo8 = n < 8;
                st16(c, e2a, lo8 ? ov[0] : r1);
                st16(c, e2b, lo8 ? r1 : ov[0]);
            }
            if (w == 0) {
                const v4u ones = {0x3f803f80u, 0x3f803f80u, 0x3f803f80u, 0x3f803f80u};
                f32x4 l = Z4;
#pragma unroll
                for (int s = 0; s < 4; ++s) l = mfma(ones, pf[s], l);
                if (g == 0) st8(c, WS_ST + ((bb * 32 + h) * 64 + split) * 8, v2u{tagf<T>(M), tagf<T>(l.x)});
            }
            STAMP(4 + 20 * layer);
            if constexpr (layer == 0) {
                if (w >= 4) wo_rows(w);
            }
        }
        v4u wvo[2][4];
        if constexpr (layer == 0) {
            IDS();
            if (w >= 4) asm volatile("s_waitcnt vmcnt(0)" ::: "memory");
            v2u tr[4][8];
            const int q = n >> 2, pp = n & 3;
#pragma unroll
            for (int t = 0; t < 2; ++t) {
                const int d = 8 * g + 4 * t + q;
#pragma unroll
                for (int nt = 0; nt < 4; ++nt) {
                    const unsigned ad = lds0 + w * 16384 + d * 128 + (((2 * nt + (pp >> 1)) ^ swz8(d)) * 16) + 8 * (pp & 1);
                    TR_ISSUE(tr[nt][0 + t], ad, 0); TR_ISSUE(tr[nt][2 + t], ad, 4096); TR_ISSUE(tr[nt][4 + t], ad, 8192); TR_ISSUE(tr[nt][6 + t], ad, 12288);
                }
            }
            asm volatile("s_waitcnt lgkmcnt(0)" : "+v"(tr[0][0]), "+v"(tr[0][1]), "+v"(tr[0][2]), "+v"(tr[0][3]), "+v"(tr[0][4]), "+v"(tr[0][5]), "+v"(tr[0][6]), "+v"(tr[0][7]),
                         "+v"(tr[1][0]), "+v"(tr[1][1]), "+v"(tr[1][2]), "+v"(tr[1][3]), "+v"(tr[1][4]), "+v"(tr[1][5]), "+v"(tr[1][6]), "+v"(tr[1][7]) :: "memory");
            asm volatile("" : "+v"(tr[2][0]), "+v"(tr[2][1]), "+v"(tr[2][2]), "+v"(tr[2][3]), "+v"(tr[2][4]), "+v"(tr[2][5]), "+v"(tr[2][6]), "+v"(tr[2][7]),
                         "+v"(tr[3][0]), "+v"(tr[3][1]), "+v"(tr[3][2]), "+v"(tr[3][3]), "+v"(tr[3][4]), "+v"(tr[3][5]), "+v"(tr[3][6]), "+v"(tr[3][7]) :: "memory");
#pragma unroll
            for (int ks = 0; ks < 2; ++ks)
#pragma unroll
                for (int ct = 0; ct < 4; ++ct) *(LAS v4u*)(L + w * 16384 + 8192 + (ks * 4 + ct) * 1024 + lane * 16) = wqk[ks][ct];
            const int fs = swz16(n);
#pragma unroll
            for (int ks = 0; ks < 2; ++ks) {
                v4u u0[4], u1[4];
#pragma unroll
                for (int s = 0; s < 4; ++s) {
                    u0[s] = *(LAS v4u*)(L + L_A + (32 * ks + n) * 256 + (((4 * s + g) ^ fs) * 16));
                    u1[s] = *(LAS v4u*)(L + L_A + (32 * ks + 16 + n) * 256 + (((4 * s + g) ^ fs) * 16));
                }
#pragma unroll
                for (int nt = 0; nt < 4; ++nt) {
                    f32x4 a0 = Z4, a1 = Z4;
#pragma unroll
                    for (int s = 0; s < 4; ++s) {
                        const v4u bo = v4u{tr[nt][2 * s].x, tr[nt][2 * s].y, tr[nt][2 * s + 1].x, tr[nt][2 * s + 1].y};
                        a0 = mfma(u0[s], bo, a0); a1 = mfma(u1[s], bo, a1);
                    }
                    *(LAS v4u*)(L + w * 16384 + (ks * 4 + nt) * 1024 + lane * 16) =
                        v4u{pack2(a0.x, a0.y), pack2(a0.z, a0.w), pack2(a1.x, a1.y), pack2(a1.z, a1.w)};
                }
                FENCE();
            }
            if (w < 4) {
                    const u16* pb = tid < 56 ? p.w2 + (size_t)(56 * idx + tid) * MD + 64 * xcd
                                             : ((tid < 136 ? p.w1 : p.w3) + (size_t)(16 * idx + ((tid - 56) % 80) / 5) * FF + min(224 * xcd + 64 * (((tid - 56) % 80) % 5), FF - 2));
                    asm volatile("global_load_dword %0, %1, off" : "=v"(warm) : "v"(pb) : "memory");
            }
            __syncthreads();
        }
        STAMP(5 + 20 * layer);
        {
            IDS();
#pragma unroll
            for (int ks = 0; ks < 2; ++ks)
#pragma unroll
                for (int nt = 0; nt < 4; ++nt) wvo[ks][nt] = *(LAS v4u*)(L + w * 16384 + (ks * 4 + nt) * 1024 + lane * 16);
            if constexpr (layer == 1) nap(D_E2);
            const int b = w >> 2, chunk = lane & 7, sp0 = (tid >> 3) & 31;
            const int ib = WS_E2 + (h1 * 8 + j1) * 16384 + (b * 64 + sp0) * 128 + chunk * 16;
            const int sb = WS_ST + ((b * 32 + h1) * 64 + lane) * 8;
            const FragAddr FA = frag_addr<false>(w, lane);
            int imo = L_A + (b * 64 + sp0) * 128 + ((chunk ^ swz8(sp0)) * 16);
            int tra = (int)lds0 + L_A + (b * 64 + 8 * g + (n >> 2)) * 128 + (((2 * (w & 3) + ((n & 3) >> 1)) ^ swz8(8 * g + (n >> 2))) * 16) + 8 * (n & 1);
            int x2w = L_X2 + (b * 32 + 8 * (w & 3) + (n >> 1)) * 4;
            int ofr = (n < 4 ? L_X2 + n * 128 : L_CST + 128) + 16 * g;
            int e3d = WS_E3 + 4 * w * 32768 + cu * 128 + n * 8;
            pini(imo); pini(tra); pini(x2w); pini(ofr); pini(e3d);
            v4u d0, d1; v2u ls;
            int spins = 0;
            for (;;) {
                d0 = ld16(c, ib); d1 = ld16(c, ib + 32 * 128); ls = ld8(c, sb);
                if (__all(okbit<T>(andor<T>(andor<T>(v4<T>(d0), v4<T>(d1)), andor<T>(ls.x, ls.y))))) break;
                if (give_up(c, spins)) break;
            }
            STAMP(6 + 20 * layer);
            if constexpr (layer == 0) {
                asm volatile("" ::"v"(warm));
                const int gq = isGU ? gu : 0;
#pragma unroll
                for (int mt = 0; mt < 4; ++mt) {
                    const u16* pa = p.w1 + (size_t)(64 * w + 16 * mt + n) * FF + 32 * gq + 8 * g;
                    const u16* pb = p.w3 + (size_t)(64 * w + 16 * mt + n) * FF + 32 * gq + 8 * g;
                    if (isGU) { glf[0][mt] = ldnt(pa); glf[1][mt] = ldnt(pb); }
                }
                FENCE();
            }
            *(LAS v4u*)(L + imo) = d0; *(LAS v4u*)(L + imo + 32 * 128) = d1;
            const float Mi = u2f(ls.x);
            const float e = __builtin_amdgcn_exp2f(Mi - wave_max(Mi));
            const float wn = e * __builtin_amdgcn_rcpf(wave_sum(e * u2f(ls.y)));
            v4u wf[2];
            mk_frags(wn, 0.f, L, FA, lane & 1, wf);
            __syncthreads();
            {
                v2u t0, t1, t2, t3;
                asm volatile("ds_read_b64_tr_b16 %0, %4\n\tds_read_b64_tr_b16 %1, %4 offset:512\n\tds_read_b64_tr_b16 %2, %4 offset:4096\n\tds_read_b64_tr_b16 %3, %4 offset:4608\n\ts_waitcnt lgkmcnt(0)"
                             : "=&v"(t0), "=&v"(t1), "=&v"(t2), "=&v"(t3) : "v"(tra) : "memory");
                f32x4 o = mfma(wf[0], v4u{t0.x, t0.y, t1.x, t1.y}, Z4);
                o = mfma(wf[1], v4u{t2.x, t2.y, t3.x, t3.y}, o);
                const float v = o.x + o.z;
                const float vn = dpp<0xB1>(v);
                const unsigned hi = pack2(v, vn);
                const unsigned lo = pack2(v - bflo(hi), vn - bfhi(hi));
                LAS unsigned* x2 = (LAS unsigned*)(L + x2w);
                if ((lane & 0x31) == 0) { x2[0] = hi; x2[64] = lo; }
            }
            __syncthreads();
            const v4u of0 = *(LAS v4u*)(L + ofr), of1 = *(LAS v4u*)(L + ofr + 64);
            f32x4 a2[4];
#pragma unroll
            for (int nt = 0; nt < 4; ++nt) a2[nt] = mfma(of0, wvo[0][nt], Z4);
#pragma unroll
            for (int nt = 0; nt < 4; ++nt) a2[nt] = mfma(of1, wvo[1][nt], a2[nt]);
            if (g == 0) {
#pragma unroll
                for (int nt = 0; nt < 4; ++nt) st8(c, e3d + nt * 32768, v2u{tagf<T>(a2[nt].x + a2[nt].z), tagf<T>(a2[nt].y + a2[nt].w)});
            }
            STAMP(7 + 20 * layer);
        }
        auto ident = [&](int n, int g, int hi) {
            unsigned a[4];
#pragma unroll
            for (int q = 0; q < 4; ++q) { const int k0 = 8 * g + 2 * q - 16 * hi; a[q] = (k0 == n ? 0x3f80u : 0u) | (k0 + 1 == n ? 0x3f800000u : 0u); }
            return v4u{a[0], a[1], a[2], a[3]};
        };
        if (isRed) {
            IDS();
            nap(D_E3);
            const int ib = WS_E3 + red * 32768 + tid * 16;
            int rdw = L_RED + w * 128 + lane * 16, rdr = L_RED + (lane & 7) * 128 + (lane >> 3) * 16, e4d = WS_E4 + red * 128 + (lane >> 3) * 16;
            pini(rdw); pini(rdr); pini(e4d);
            v4u d[4];
            {
                D4x16 dd;
                poll2(c, dd, [&](D4x16& q) {
#pragma unroll
                    for (int k = 0; k < 4; ++k) q.d[k] = ld16(c, ib + k * 8192); },
                    [&](const D4x16& q) { return okbit<T>(andor<T>(andor<T>(v4<T>(q.d[0]), v4<T>(q.d[1])), andor<T>(v4<T>(q.d[2]), v4<T>(q.d[3])))); });
#pragma unroll
                for (int k = 0; k < 4; ++k) d[k] = dd.d[k];
            }
            STAMP(8 + 20 * layer);
            float a[4];
            a[0] = (u2f(d[0].x) + u2f(d[1].x)) + (u2f(d[2].x) + u2f(d[3].x));
            a[1] = (u2f(d[0].y) + u2f(d[1].y)) + (u2f(d[2].y) + u2f(d[3].y));
            a[2] = (u2f(d[0].z) + u2f(d[1].z)) + (u2f(d[2].z) + u2f(d[3].z));
            a[3] = (u2f(d[0].w) + u2f(d[1].w)) + (u2f(d[2].w) + u2f(d[3].w));
#pragma unroll
            for (int i = 0; i < 4; ++i) { a[i] += dpp<0x128>(a[i]); a[i] = x32_sum(x16_sum(a[i])); }
            if (lane < 8) *(LAS f32x4*)(L + rdw) = f32x4{a[0], a[1], a[2], a[3]};
            __syncthreads();
            if (w == 0) {
                const f32x4 v = *(LAS f32x4*)(L + rdr);
                xres += f32x4{sum8(v.x), sum8(v.y), sum8(v.z), sum8(v.w)};
                if ((lane & 7) == 0) st16(c, e4d, v4u{tagf<T>(xres.x), tagf<T>(xres.y), tagf<T>(xres.z), tagf<T>(xres.w)});
            }
            STAMP(9 + 20 * layer);
            if constexpr (layer == 0) {
                const u16* pw = p.w2 + (size_t)(224 * w + 16 * (g >> 1) + n) * MD + 16 * red + 8 * (g & 1);
                const v4u Ilo = ident(n, g, 0), Ihi = ident(n, g, 1);
                v4u raw[7];
#pragma unroll
                for (int ks = 0; ks < 7; ++ks) raw[ks] = ldnt(pw + (size_t)ks * 32 * MD);
                FENCE();
#pragma unroll
                for (int ks = 0; ks < 7; ++ks) {
                    const f32x4 a0 = mfma(raw[ks], Ilo, Z4), a1 = mfma(raw[ks], Ihi, Z4);
                    glf[ks >> 2][ks & 3] = v4u{pack2(a0.x, a0.y), pack2(a0.z, a0.w), pack2(a1.x, a1.y), pack2(a1.z, a1.w)};
                    pin(glf[ks >> 2][ks & 3]);
                }
            }
        }
        if (isGU) {
            IDS();
            if constexpr (layer == 0) {
                const v4u Ilo = ident(n, g, 0), Ihi = ident(n, g, 1);
                v4u raw[2][4];
#pragma unroll
                for (int mt = 0; mt < 4; ++mt) { raw[0][mt] = glf[0][mt]; raw[1][mt] = glf[1][mt]; }
#pragma unroll
                for (int mat = 0; mat < 2; ++mat)
#pragma unroll
                    for (int ks = 0; ks < 2; ++ks) {
                        const f32x4 l0 = mfma(raw[mat][2 * ks], Ilo, Z4), h0 = mfma(raw[mat][2 * ks], Ihi, Z4);
                        const f32x4 l1 = mfma(raw[mat][2 * ks + 1], Ilo, Z4), hh1 = mfma(raw[mat][2 * ks + 1], Ihi, Z4);
                        glf[ks][mat * 2] = v4u{pack2(l0.x, l0.y), pack2(l0.z, l0.w), pack2(l1.x, l1.y), pack2(l1.z, l1.w)};
                        glf[ks][mat * 2 + 1] = v4u{pack2(h0.x, h0.y), pack2(h0.z, h0.w), pack2(hh1.x, hh1.y), pack2(hh1.z, hh1.w)};
                        pin(glf[ks][mat * 2]); pin(glf[ks][mat * 2 + 1]);
                    }
            } else nap(D_E4);
            const int pair = 4 * (w & 3) + ((lane >> 3) & 3) + 16 * (lane >> 5);
            const GemvAddr GA = gemv_addr(w, lane, pair);
            int e5d = WS_E5 + gu * 256 + (pair & 15) * 16;
            pini(e5d);
            float x0, x1;
            {
                D2x8 dd;
                poll2(c, dd, [&](D2x8& d) { d.v = ld8(c, WS_E4 + tid * 8); d.s = ld8(c, WS_E4); },
                      [&](const D2x8& d) { return okbit<T>(andor<T>(andor<T>(d.v.x, d.v.y), andor<T>(d.s.x, d.s.y))); });
                x0 = u2f(dd.v.x) - u2f(dd.s.x); x1 = u2f(dd.v.y) - u2f(dd.s.y);
            }
            STAMP(10 + 20 * layer);
            ln_gemv_partials(x0, x1, glf, L, GA, lane);
            __syncthreads();
            if (w < 4) {
                float o[4];
                ln_gemv_final(L, GA, o);
                float hv[4];
#pragma unroll
                for (int i = 0; i < 4; ++i) {
                    auto r = __builtin_amdgcn_permlane32_swap(f2u(o[i]), f2u(o[i]), false, false);
                    const float gt = u2f(r[0]), up = u2f(r[1]);
                    hv[i] = gt * __builtin_amdgcn_rcpf(1.0f + __builtin_amdgcn_exp2f(-1.4426950408889634f * gt)) * up;
                }
                if ((lane & 0x27) == 0) st16(c, e5d, v4u{tagf<T>(hv[0]), tagf<T>(hv[1]), tagf<T>(hv[2]), tagf<T>(hv[3])});
            }
            STAMP(11 + 20 * layer);
        }
        if (isRed) {
            IDS();
            if constexpr (layer == 1) nap(D_E5);
            const int ib = WS_E5 + (112 * w + lane) * 16;
            const int ib1 = ib + (lane < 48 ? 1024 : 0);
            int hw, hr = (n < 4 ? L_A + w * 1856 + n * 464 : L_CST + 128) + 16 * g;
            { const int f = 2 * lane; hw = L_A + w * 1856 + ((f & ~31) + 8 * ((f >> 2) & 3) + 4 * ((f >> 4) & 1) + (f & 3)) * 2; }
            int rdw = L_RED + w * 128 + n * 8, rdr = L_RED + (lane & 7) * 128 + (lane >> 3) * 16, e6d = WS_E6 + red * 128 + (lane >> 3) * 16;
            pini(hw); pini(hr); pini(rdw); pini(rdr); pini(e6d);
            v4u d0, d1;
            {
                D2x16 dd;
                poll2(c, dd, [&](D2x16& d) { d.d0 = ld16(c, ib); d.d1 = ld16(c, ib1); }, [&](const D2x16& d) { return okbit<T>(andor<T>(v4<T>(d.d0), v4<T>(d.d1))); });
                d0 = dd.d0; d1 = dd.d1;
            }
            STAMP(12 + 20 * layer);
#pragma unroll
            for (int k = 0; k < 2; ++k) {
                const v4u d = k ? d1 : d0;
                const unsigned h0 = pack2(u2f(d.x), u2f(d.z)), hb1 = pack2(u2f(d.y), u2f(d.w));
                const unsigned l0 = pack2(u2f(d.x) - bflo(h0), u2f(d.z) - bfhi(h0)), lb1 = pack2(u2f(d.y) - bflo(hb1), u2f(d.w) - bfhi(hb1));
                LAS unsigned char* hs = L + hw + 256 * k;
                if (k == 0 || lane < 48) {
                    *(LAS unsigned*)hs = h0; *(LAS unsigned*)(hs + 464) = hb1;
                    *(LAS unsigned*)(hs + 928) = l0; *(LAS unsigned*)(hs + 1392) = lb1;
                }
            }
            f32x4 a = Z4, a2 = Z4;
#pragma unroll
            for (int ks = 0; ks < 7; ++ks) {
                const v4u v = *(LAS v4u*)(L + hr + 64 * ks);
                if (ks & 1) a2 = mfma(v, glf[ks >> 2][ks & 3], a2); else a = mfma(v, glf[ks >> 2][ks & 3], a);
            }
            a += a2;
            if (g == 0) *(LAS f32x2*)(L + rdw) = f32x2{a.x + a.z, a.y + a.w};
            __syncthreads();
            if (w == 0) {
                const f32x4 v = *(LAS f32x4*)(L + rdr);
                xres += f32x4{sum8(v.x), sum8(v.y), sum8(v.z), sum8(v.w)};
                if ((lane & 7) == 0) {
                    if constexpr (layer == 0) st16(c, e6d, v4u{tagf<1u>(xres.x), tagf<1u>(xres.y), tagf<1u>(xres.z), tagf<1u>(xres.w)});
                    else {
                        GAS unsigned* po = (GAS unsigned*)(p.out + 16 * red + 2 * (lane >> 3));
                        po[0] = pack2(xres.x, xres.z); po[256] = pack2(xres.y, xres.w);
                    }
                }
            }
            STAMP(13 + 20 * layer);
        }
    };
    layer_body(std::integral_constant<int, 0>{});
    layer_body(std::integral_constant<int, 1>{});
    if (cu == 0 && w == 0) {
        const unsigned long long d = __builtin_amdgcn_s_memrealtime();
        while (__builtin_amdgcn_s_memrealtime() - d < 10ull * tq_) __builtin_amdgcn_s_sleep(1);
    }
    asm volatile("mk_code_end:");
}
}

extern "C" void kernel_launch(void* const* d_in, const int* in_sizes, int n_in,
                              void* d_out, int out_size, void* d_ws, size_t ws_size,
                              hipStream_t stream) {
    mk::Params p;
    p.x = (const u16*)d_in[0]; p.ckv = (const u16*)d_in[1]; p.wq = (const u16*)d_in[2]; p.wuk = (const u16*)d_in[3];
    p.wuv = (const u16*)d_in[4]; p.wo = (const u16*)d_in[5]; p.w1 = (const u16*)d_in[7]; p.w3 = (const u16*)d_in[8];
    p.w2 = (const u16*)d_in[9]; p.out = (u16*)d_out; p.ws = (unsigned char*)d_ws;
    (void)hipMemsetAsync(d_ws, 0, mk::WS_ZERO_BYTES, stream);
    hipLaunchKernelGGL(mk::mega, dim3(mk::NWG), dim3(mk::NT), 0, stream, p);
}
```

```cpp
#include <hip/hip_runtime.h>
#include <hip/hip_bf16.h>
#include <type_traits>

typedef unsigned short u16;
typedef unsigned v4u __attribute__((ext_vector_type(4)));
typedef unsigned v2u __attribute__((ext_vector_type(2)));
typedef float f32x4 __attribute__((ext_vector_type(4)));
typedef float f32x2 __attribute__((ext_vector_type(2)));
typedef short bf16x8 __attribute__((ext_vector_type(8)));
typedef __bf16 bf16x2 __attribute__((ext_vector_type(2)));
#define GAS __attribute__((address_space(1)))
#define LAS __attribute__((address_space(3)))
#define DI __device__ __forceinline__
#define RLX_AGENT __ATOMIC_RELAXED, __HIP_MEMORY_SCOPE_AGENT

#define STAMPS 0
#define CKV_A 4

namespace mk {
constexpr int NWG = 256, NT = 512;
constexpr int SK = 8192, DC = 512, MD = 512, HD = 4096, FF = 1792;
constexpr int WS_E1 = 256;
constexpr int WS_ST = WS_E1 + 65536;
constexpr int WS_E4 = WS_ST + 32768;
constexpr int WS_E5 = WS_E4 + 4096;
constexpr int WS_E6 = WS_E5 + 14336;
constexpr int WS_HM = 262144;
constexpr int WS_HR = WS_HM + 4096;
constexpr int WS_HW = WS_HR + 4096;
constexpr int WS_E2 = 1 << 20;
constexpr int WS_E3 = WS_E2 + 256 * 16384;
constexpr int WS_STAMP = WS_E3 + 32 * 32768;
constexpr int WS_BYTES = WS_STAMP + 256 * 64 * 8;
static_assert(WS_E6 + 4096 <= WS_HM && WS_HW + 32768 <= WS_E2, "ws map");
constexpr int L_A = 131072;
constexpr int L_RED = L_A + 17408;
constexpr int L_P = L_RED;
constexpr int L_SMAX = L_RED + 6400;
constexpr int L_XS = L_SMAX + 512;
constexpr int L_X2 = L_XS + 4096;
constexpr int L_CST = L_X2 + 512;
constexpr int L_GO = L_CST + 640;
constexpr int L_BYTES = 163840;
static_assert(L_CST + 656 <= L_BYTES && L_CST >= L_A + 7 * 4096, "LDS");

struct Params { const u16 *x, *ckv, *wq, *wuk, *wuv, *wo, *w1, *w3, *w2; u16* out; unsigned char* ws; };

DI float u2f(unsigned u) { return __builtin_bit_cast(float, u); }
DI unsigned f2u(float f) { return __builtin_bit_cast(unsigned, f); }
DI f32x4 mfma(v4u a, v4u b, f32x4 c) {
    return __builtin_amdgcn_mfma_f32_16x16x32_bf16(__builtin_bit_cast(bf16x8, a), __builtin_bit_cast(bf16x8, b), c, 0, 0, 0);
}
DI unsigned pack2(float a, float b) { bf16x2 v = {(__bf16)a, (__bf16)b}; return __builtin_bit_cast(unsigned, v); }
DI float bflo(unsigned u) { return u2f(u << 16); }
DI float bfhi(unsigned u) { return u2f(u & 0xffff0000u); }
template <unsigned T> DI unsigned pack2_tag(float a, float b) {
    const unsigned u = f2u(a);
    const unsigned lo = T ? ((u >> 16) | 1u) : (((u + 0x10000u) >> 17) << 1);
    return __builtin_amdgcn_perm(pack2(b, b), lo, 0x07060100u);
}
template <unsigned T> DI unsigned tagf(float a) { return T ? (f2u(a) | 1u) : (f2u(a) & ~1u); }
DI v4u ldnt(const u16* p) { return __builtin_nontemporal_load((const GAS v4u*)p); }
DI void glds16(const void* gsrc, unsigned lds_dst) {
    unsigned keep;
    asm volatile("s_mov_b32 %0, m0\n\ts_mov_b32 m0, %2\n\ts_nop 0\n\tglobal_load_lds_dwordx4 %1, off nt\n\ts_mov_b32 m0, %0" : "=&s"(keep) : "v"(gsrc), "s"(lds_dst) : "memory");
}
#define LDNT_ASM(dst, ptr) asm volatile("global_load_dwordx4 %0, %1, off nt" : "=v"(dst) : "v"(ptr) : "memory")
DI int swz16(int r) { return ((r >> 3) & 1) | ((r & 3) << 1) | ((((r >> 2) ^ (r >> 3)) & 1) << 3); }
DI int swz8(int r) { return (r & 2) | ((r >> 1) & 4); }
#define TR_ISSUE(dst, addr, OFF) asm volatile("ds_read_b64_tr_b16 %0, %1 offset:" #OFF : "=v"(dst) : "v"(addr) : "memory")

template <int CTRL> DI unsigned dppu(unsigned v) { return (unsigned)__builtin_amdgcn_update_dpp(0, (int)v, CTRL, 0xf, 0xf, true); }
template <int CTRL> DI float dpp(float v) { return u2f(dppu<CTRL>(f2u(v))); }
DI float row_sum16(float v) { v += dpp<0xB1>(v); v += dpp<0x4E>(v); v += dpp<0x141>(v); v += dpp<0x140>(v); return v; }
DI float row_max16(float v) { v = fmaxf(v, dpp<0xB1>(v)); v = fmaxf(v, dpp<0x4E>(v)); v = fmaxf(v, dpp<0x141>(v)); v = fmaxf(v, dpp<0x140>(v)); return v; }
DI float x16_sum(float v) { auto r = __builtin_amdgcn_permlane16_swap(f2u(v), f2u(v), false, false); return u2f(r[0]) + u2f(r[1]); }
DI float x32_sum(float v) { auto r = __builtin_amdgcn_permlane32_swap(f2u(v), f2u(v), false, false); return u2f(r[0]) + u2f(r[1]); }
DI float x16_max(float v) { auto r = __builtin_amdgcn_permlane16_swap(f2u(v), f2u(v), false, false); return fmaxf(u2f(r[0]), u2f(r[1])); }
DI float x32_max(float v) { auto r = __builtin_amdgcn_permlane32_swap(f2u(v), f2u(v), false, false); return fmaxf(u2f(r[0]), u2f(r[1])); }
DI float wave_sum(float v) { return x32_sum(x16_sum(row_sum16(v))); }
DI float wave_max(float v) { return x32_max(x16_max(row_max16(v))); }
DI void pin(v4u& v) { asm volatile("" : "+v"(v)); }
DI void nap(int n) {
#pragma unroll 1
    for (int i = 0; i < n; ++i) __builtin_amdgcn_s_sleep(1);
}

struct Ctx {
    __amdgpu_buffer_rsrc_t rs;
    unsigned long long t0;
    bool dead;
};
DI v4u ld16(const Ctx& c, int off) { return __builtin_amdgcn_raw_buffer_load_b128(c.rs, off, 0, 16); }
DI v2u ld8(const Ctx& c, int off) { return __builtin_amdgcn_raw_buffer_load_b64(c.rs, off, 0, 16); }
DI void st16(const Ctx& c, int off, v4u v) { __builtin_amdgcn_raw_buffer_store_b128(v, c.rs, off, 0, 16); }
DI void st8(const Ctx& c, int off, v2u v) { __builtin_amdgcn_raw_buffer_store_b64(v, c.rs, off, 0, 16); }
DI void st4(const Ctx& c, int off, unsigned v) { __builtin_amdgcn_raw_buffer_store_b32(v, c.rs, off, 0, 16); }
DI bool give_up(Ctx& c, int& spins) {
    if (c.dead) return true;
    if ((++spins & 63) == 0 && (long long)(__builtin_amdgcn_s_memrealtime() - c.t0) > 2000000ll) { c.dead = true; return true; }
    return false;
}
DI unsigned ld4(const Ctx& c, int off) { return __builtin_amdgcn_raw_buffer_load_b32(c.rs, off, 0, 16); }
template <unsigned T> DI unsigned andor(unsigned a, unsigned b) { return T ? (a & b) : (a | b); }
template <unsigned T> DI unsigned v4(v4u v) { return andor<T>(andor<T>(v.x, v.y), andor<T>(v.z, v.w)); }
template <unsigned T> DI bool okbit(unsigned m) { return (m & 1u) == T; }

DI void pini(int& v) { asm volatile("" : "+v"(v)); }
template <class D, class LD, class OK> DI void poll2(Ctx& c, D& out, LD ld, OK ok) {
    int spins = 0;
    for (;;) {
        ld(out);
        if (__all(ok(out))) return;
        if (give_up(c, spins)) return;
    }
}
struct D2x8 { v2u v, s; };
struct D4x16 { v4u d[4]; };
struct D2x16 { v4u d0, d1; };
struct FragAddr { int wr, rd; };
template <bool PERM> DI FragAddr frag_addr(int w, int lane) {
    const int n = lane & 15, g = lane >> 4;
    const int pos2 = PERM ? (16 * (lane >> 5) + 4 * ((lane >> 2) & 3) + 2 * ((lane >> 4) & 1) + ((lane >> 1) & 1)) : (lane >> 1);
    FragAddr f;
    f.wr = L_XS + w * 512 + ((lane & 1) ? 256 : 0) + pos2 * 4;
    f.rd = (n < 4 ? L_XS + w * 512 + n * 128 : (n == 4 ? L_CST : L_CST + 128)) + 16 * g;
    pini(f.wr); pini(f.rd);
    return f;
}
DI void mk_frags(float v0, float v1, LAS unsigned char* L, const FragAddr& f, bool odd, v4u (&fr)[2]) {
    const unsigned hi = pack2(v0, v1);
    const unsigned lo = pack2(v0 - bflo(hi), v1 - bfhi(hi));
    const unsigned recv = dppu<0xB1>(odd ? hi : lo);
    const unsigned a = odd ? recv : hi, b = odd ? lo : recv;
    LAS unsigned* x32 = (LAS unsigned*)(L + f.wr);
    x32[0] = __builtin_amdgcn_perm(b, a, 0x05040100u); x32[32] = __builtin_amdgcn_perm(b, a, 0x07060302u);
    fr[0] = *(LAS v4u*)(L + f.rd); fr[1] = *(LAS v4u*)(L + f.rd + 64);
}
DI float sum8(float v) { v += dpp<0xB1>(v); v += dpp<0x4E>(v); v += dpp<0x141>(v); return v; }

#define IDS() int tid = threadIdx.x; asm volatile("" : "+v"(tid)); const int lane = tid & 63, n = lane & 15, g = lane >> 4; (void)n; (void)g; (void)lane
#define FENCE() __builtin_amdgcn_sched_barrier(0)
#define STAMP(i) do {} while (0)

#define D_E1A 0
#define D_E1 16
#define D_E2 40
#define D_E3 20
#define D_E4 70
#define D_E5 50
#define D_E6 130
#define D_E6R 14
#define D_E6G 50

struct GemvAddr { FragAddr f; int wq, ws, rq, rc, rs; };
DI GemvAddr gemv_addr(int w, int lane, int pair  ) {
    const int n = lane & 15, g = lane >> 4;
    GemvAddr a;
    a.f = frag_addr<true>(w, lane);
    a.wq = L_RED + w * 768 + (g == 0 ? n * 8 : 512 + n * 4);
    a.ws = L_RED + 6144 + w * 16 + (lane == 4 ? 0 : 8 + lane * 4);
    a.rq = L_RED + (lane & 7) * 768 + pair * 16;
    a.rc = L_RED + (lane & 7) * 768 + 512 + pair * 8;
    a.rs = L_RED + 6144 + (lane & 7) * 16;
    pini(a.wq); pini(a.ws); pini(a.rq); pini(a.rc); pini(a.rs);
    return a;
}
DI void ln_gemv_partials(float x0, float x1, const v4u (&bf)[2][4], LAS unsigned char* L, const GemvAddr& A, int lane) {
    const int g = lane >> 4;
    v4u xf[2];
    mk_frags(x0, x1, L, A.f, lane & 1, xf);
    const f32x4 Z = {0.f, 0.f, 0.f, 0.f};
    f32x4 a[4], gm;
#pragma unroll
    for (int ct = 0; ct < 4; ++ct) a[ct] = mfma(xf[0], bf[0][ct], Z);
    gm = mfma(xf[0], xf[0], Z);
#pragma unroll
    for (int ct = 0; ct < 4; ++ct) a[ct] = mfma(xf[1], bf[1][ct], a[ct]);
    gm = mfma(xf[1], xf[1], gm);
    if (g == 0) {
#pragma unroll
        for (int ct = 0; ct < 4; ++ct) *(LAS f32x2*)(L + A.wq + ct * 128) = f32x2{a[ct].x + a[ct].z, a[ct].y + a[ct].w};
        if (lane == 4) *(LAS f32x2*)(L + A.ws) = f32x2{gm.x + gm.z, gm.y + gm.w};
        if (lane < 2) *(LAS float*)(L + A.ws) = lane == 0 ? gm.x + 2.f * gm.z : gm.y + 2.f * gm.w;
    } else if (g == 1) {
#pragma unroll
        for (int ct = 0; ct < 4; ++ct) *(LAS float*)(L + A.wq + ct * 64) = a[ct].x;
    }
}
DI void ln_gemv_final(LAS unsigned char* L, const GemvAddr& A, float sh0, float sh1, float (&o)[4]) {
    f32x4 q = *(LAS f32x4*)(L + A.rq), st = *(LAS f32x4*)(L + A.rs);
    f32x2 cs = *(LAS f32x2*)(L + A.rc);
    st.x = sum8(st.x); st.y = sum8(st.y); st.z = sum8(st.z); st.w = sum8(st.w);
    q.x = sum8(q.x); q.y = sum8(q.y); q.z = sum8(q.z); q.w = sum8(q.w);
    cs.x = sum8(cs.x); cs.y = sum8(cs.y);
    const float m0 = st.x * (1.f / 512), m1 = st.y * (1.f / 512);
    const unsigned mb = pack2(m0 + sh0, m1 + sh1);
    const unsigned vb = pack2(fmaxf(st.z * (1.f / 512) - m0 * m0, 0.f), fmaxf(st.w * (1.f / 512) - m1 * m1, 0.f));
    const unsigned tb = pack2(bflo(vb) + 1e-5f, bfhi(vb) + 1e-5f);
    const unsigned sd = pack2(__builtin_amdgcn_sqrtf(bflo(tb)), __builtin_amdgcn_sqrtf(bfhi(tb)));
    const float r0 = __builtin_amdgcn_rcpf(bflo(sd)), r1 = __builtin_amdgcn_rcpf(bfhi(sd));
    const float e0 = bflo(mb) - sh0, e1 = bfhi(mb) - sh1;
    o[0] = (q.x - e0 * cs.x) * r0; o[1] = (q.y - e1 * cs.x) * r1;
    o[2] = (q.z - e0 * cs.y) * r0; o[3] = (q.w - e1 * cs.y) * r1;
}

__global__ void __launch_bounds__(NT, 2) mega(Params p) {
    __shared__ __attribute__((aligned(16))) unsigned char smem[L_BYTES];
    LAS unsigned char* L = (LAS unsigned char*)smem;
    const int tid = threadIdx.x, lane = tid & 63, w = __builtin_amdgcn_readfirstlane(tid >> 6), n = lane & 15, g = lane >> 4;
    const int cu = blockIdx.x, xcd = cu & 7, idx = cu >> 3;
    const int h1 = xcd * 4 + (idx >> 3), j1 = idx & 7;
    const int gs = xcd * 16 + ((idx & 7) | ((idx >> 4) << 3)), bb = gs >> 6, split = gs & 63, hg = 1 - ((idx >> 3) & 1);
    const bool isRed = idx < 4, isGU = idx >= 8 && idx < 15;
    const int red = xcd * 4 + idx, gu = xcd * 7 + (idx - 8);
    Ctx c;
    c.rs = __builtin_amdgcn_make_buffer_rsrc((void*)p.ws, 0, WS_BYTES, 0x00020000);
    c.t0 = __builtin_amdgcn_s_memrealtime();
    c.dead = false;
    unsigned hc = 0, h0 = 0, hr = 0;
    unsigned mo[4], mw[4];
#pragma unroll
    for (int k = 0; k < 4; ++k) { mo[k] = 0; mw[k] = 0; }
    const GAS unsigned char* wsb = (const GAS unsigned char*)p.ws;
    if (w == 6) {
        const int e1o = WS_E1 + ((h1 & 1) * 16 + (h1 >> 1)) * 1024 + 128 * j1 + (lane >> 5) * 32768 + (lane & 31) * 4;
        st4(c, e1o, 0u);
        if (lane < 16) st8(c, WS_ST + ((bb * 32 + 2 * lane + hg) * 64 + split) * 8, v2u{0u, 0u});
        if (isRed) st4(c, (lane < 32 ? WS_E4 : WS_E6 - 128) + red * 128 + lane * 4, 0u);
        if (isGU) st4(c, WS_E5 + gu * 256 + lane * 4, 0u);
        asm volatile("global_load_dword %0, %2, off offset:4 sc1\n\tglobal_load_dword %1, %3, off sc1"
                     : "=&v"(hc), "=&v"(hr) : "v"(wsb + WS_HW + cu * 128), "v"(wsb + WS_HR + cu * 4) : "memory");
    }
    if (w == 7 && cu == 0) {
        asm volatile("global_load_dword %0, %8, off sc1\n\tglobal_load_dword %1, %8, off offset:256 sc1\n\tglobal_load_dword %2, %8, off offset:512 sc1\n\tglobal_load_dword %3, %8, off offset:768 sc1\n\t"
                     "global_load_dword %4, %9, off sc1\n\tglobal_load_dword %5, %10, off sc1\n\tglobal_load_dword %6, %11, off sc1\n\tglobal_load_dword %7, %12, off sc1"
                     : "=&v"(mo[0]), "=&v"(mo[1]), "=&v"(mo[2]), "=&v"(mo[3]), "=&v"(mw[0]), "=&v"(mw[1]), "=&v"(mw[2]), "=&v"(mw[3])
                     : "v"(wsb + WS_HM + lane * 4), "v"(wsb + WS_HW + lane * 128), "v"(wsb + WS_HW + (lane + 64) * 128), "v"(wsb + WS_HW + (lane + 128) * 128), "v"(wsb + WS_HW + (lane + 192) * 128) : "memory");
    }
    unsigned cwarm = 0;
    {
        unsigned long long pc; unsigned len;
        asm volatile("mk_code_begin:\n\ts_getpc_b64 %0\n\ts_mov_b32 %1, mk_code_end-mk_code_begin" : "=s"(pc), "=s"(len));
        const unsigned long long ca = pc + (((unsigned)tid * 128u < len) ? (unsigned)tid * 128u : 0u);
        asm volatile("global_load_dword %0, %1, off" : "=v"(cwarm) : "v"(ca) : "memory");
    }
    STAMP(0);
    const f32x4 Z4 = {0.f, 0.f, 0.f, 0.f};

    v4u ck[16];
    v4u ckT[4][4];
    v4u glf[2][4];
    const u16* ckv0 = p.ckv + ((size_t)bb * SK + (size_t)split * 128) * DC;
    unsigned xr0, xr1, xr2, xr3, xs0, xs1;
    {
        const u16* px = p.x + tid;
        const u16* pr = p.x + (16 * (red & 31) + 2 * (lane >> 3));
        asm volatile("global_load_ushort %0, %6, off\n\tglobal_load_ushort %1, %6, off offset:1024\n\tglobal_load_dword %2, %7, off\n\tglobal_load_dword %3, %7, off offset:1024\n\t"
                     "global_load_ushort %4, %8, off\n\tglobal_load_ushort %5, %8, off offset:1024"
                     : "=&v"(xr0), "=&v"(xr1), "=&v"(xr2), "=&v"(xr3), "=&v"(xs0), "=&v"(xs1) : "v"(px), "v"(pr), "v"(p.x) : "memory");
    }

    v4u wqk[2][4];
    const unsigned lds0 = (unsigned)(size_t)smem;
    auto ckv_rows = [&](int i0 = 0, int i1 = 16) {
#pragma unroll 4
        for (int i = i0; i < i1; ++i) glds16(ckv0 + (size_t)(16 * w + i) * DC + ((lane ^ swz16(i)) * 8), lds0 + (16 * w + i) * 1024);
    };
    {
        v4u aq[4][4];
#pragma unroll
        for (int i = 0; i < 2; ++i) {
            const int r = 8 * w + 4 * i + g;
            glds16(p.wuk + (size_t)(64 * j1 + r) * HD + h1 * 128 + ((n ^ swz16(r & 15)) * 8), lds0 + L_A + (8 * w + 4 * i) * 256);
        }
        {
            const u16* pq = p.wq + (size_t)(64 * w + g) * HD + h1 * 128 + 8 * n;
#pragma unroll
            for (int mt = 0; mt < 4; ++mt)
#pragma unroll
                for (int i = 0; i < 4; ++i) { const u16* pp = pq + (size_t)(16 * mt + 4 * i) * HD; LDNT_ASM(aq[mt][i], pp); }
        }
        {
            const int ism = __builtin_amdgcn_readfirstlane(cu * 8 + (7 - w));
            asm volatile("s_cmp_lg_u32 %8, 0\n\ts_cbranch_scc1 1f\n\ts_waitcnt vmcnt(25)\n1:"
                         : "+v"(mo[0]), "+v"(mo[1]), "+v"(mo[2]), "+v"(mo[3]), "+v"(mw[0]), "+v"(mw[1]), "+v"(mw[2]), "+v"(mw[3]) : "s"(ism) : "memory", "scc");
        }
#pragma unroll
        for (int k = 0; k < 4; ++k) { mo[k] += 1u; if (mo[k] == mw[k]) mo[k] += 1u; }
        if (w == 7 && cu == 0) {
#pragma unroll
            for (int k = 0; k < 4; ++k) st4(c, WS_HM + lane * 4 + k * 256, mo[k]);
        }
        asm volatile("s_waitcnt vmcnt(16)" : "+v"(xr0), "+v"(xr1), "+v"(xr2), "+v"(xr3), "+v"(xs0), "+v"(xs1), "+v"(cwarm), "+v"(hc), "+v"(hr) :: "memory");
        hc += 1u; if (hc == hr) hc += 1u;
        STAMP(14);
        __syncthreads();
        STAMP(15);
        const int fs = swz16(n);
        v4u bu[4][4];
#pragma unroll
        for (int ct = 0; ct < 4; ++ct)
#pragma unroll
            for (int s = 0; s < 4; ++s) bu[ct][s] = *(LAS v4u*)(L + L_A + (16 * ct + n) * 256 + (((4 * s + g) ^ fs) * 16));
#pragma unroll
        for (int ct = 0; ct < 4; ++ct)
#pragma unroll
            for (int s = 0; s < 4; ++s) pin(bu[ct][s]);
        __syncthreads();
        STAMP(16);
#define HM_LOAD() asm volatile("global_load_dword %0, %1, off sc1" : "=v"(h0) : "v"(wsb + WS_HM + cu * 4) : "memory")
        if (w < 7) ckv_rows(0, CKV_A);
        else {
            const v4u z = {0u, 0u, 0u, 0u};
            const int ib = WS_E2 + (h1 * 8 + j1) * 16384 + lane * 16;
#pragma unroll
            for (int i = 0; i < 16; ++i) st16(c, ib + i * 1024, z);
#pragma unroll
            for (int i = 0; i < 4; ++i) st16(c, WS_E3 + cu * 4096 + lane * 16 + i * 1024, z);
        }
        STAMP(17);
#define WAITQ(N0, N6, N7, mt) asm volatile( \
            "s_cmp_lt_u32 %4, 6\n\ts_cbranch_scc1 1f\n\ts_cmp_eq_u32 %4, 6\n\ts_cbranch_scc1 6f\n\t" \
            "s_waitcnt vmcnt(" #N7 ")\n\ts_branch 2f\n6:\n\ts_waitcnt vmcnt(" #N6 ")\n\ts_branch 2f\n1:\n\ts_waitcnt vmcnt(" #N0 ")\n2:" \
            : "+v"(aq[mt][0]), "+v"(aq[mt][1]), "+v"(aq[mt][2]), "+v"(aq[mt][3]) : "s"(w) : "memory", "scc")
        static_assert(CKV_A == 4, "wait counts below");
        LAS unsigned char* tq = L + L_A + w * 4096;
#pragma unroll
        for (int mt = 0; mt < 4; ++mt) {
            if (mt == 0) WAITQ(16, 16, 32, 0);
            else if (mt == 1) {
                WAITQ(12, 12, 28, 1);
                if (w == 6) HM_LOAD();
            }
            else if (mt == 2) WAITQ(8, 9, 24, 2);
            else {
                WAITQ(4, 0, 0, 3); STAMP(18);
                asm volatile("" : "+v"(h0));
                if (w == 6 && lane == 0) st8(c, WS_HW + cu * 128, v2u{h0, hc});
            }
#pragma unroll
            for (int i = 0; i < 4; ++i) { const int r = 4 * i + g; *(LAS v4u*)(tq + r * 256 + ((n ^ swz16(r)) * 16)) = aq[mt][i]; }
            v4u af[4];
#pragma unroll
            for (int s = 0; s < 4; ++s) af[s] = *(LAS v4u*)(tq + n * 256 + (((4 * s + g) ^ fs) * 16));
#pragma unroll
            for (int ct = 0; ct < 4; ++ct) {
                f32x4 a0 = Z4;
#pragma unroll
                for (int s = 0; s < 4; ++s) a0 = mfma(af[s], bu[ct][s], a0);
                if (mt & 1) { wqk[mt >> 1][ct].z = pack2(a0.x, a0.y); wqk[mt >> 1][ct].w = pack2(a0.z, a0.w); }
                else { wqk[mt >> 1][ct].x = pack2(a0.x, a0.y); wqk[mt >> 1][ct].y = pack2(a0.z, a0.w); }
            }
        }
        if (w < 7) ckv_rows(CKV_A, 16); else if (cu != 0) ckv_rows();
#pragma unroll
        for (int ks = 0; ks < 2; ++ks)
#pragma unroll
            for (int ct = 0; ct < 4; ++ct) pin(wqk[ks][ct]);
    }
    if (w == 7 && lane < 41) *(LAS v4u*)(L + L_CST + lane * 16) = lane < 8 ? v4u{0x3f803f80u, 0x3f803f80u, 0x3f803f80u, 0x3f803f80u} : v4u{0u, 0u, 0u, 0u};
    __syncthreads();
    STAMP(1);
    f32x4 xres = {bflo(xr2), bflo(xr3), bfhi(xr2), bfhi(xr3)};

    auto wo_rows = [&](int v) {
        const int rr = lane >> 3;
#pragma unroll 4
        for (int i = 0; i < 16; ++i) {
            const int d = 8 * i + rr;
            glds16(p.wo + (size_t)(h1 * 128 + d) * MD + 64 * v + (((lane & 7) ^ swz8(d)) * 8), lds0 + v * 16384 + i * 1024);
        }
    };
    unsigned warm = 0;
    auto layer_body = [&](auto LC) {
        constexpr int layer = decltype(LC)::value;
        constexpr unsigned T = layer == 0 ? 1u : 0u;
        {
            IDS();
            const int pair = (tid >> 3) & 31;
            const GemvAddr GA = gemv_addr(w, lane, pair);
            int dst = WS_E1 + ((h1 & 1) * 16 + (h1 >> 1)) * 1024 + (64 * j1 + 2 * pair) * 2;
            pini(dst);
            float x0, x1, sh0, sh1;
            if constexpr (layer == 0) { sh0 = bflo(xs0); sh1 = bflo(xs1); x0 = bflo(xr0) - sh0; x1 = bflo(xr1) - sh1; }
            else {
#pragma unroll
                for (int ks = 0; ks < 2; ++ks)
#pragma unroll
                    for (int ct = 0; ct < 4; ++ct) wqk[ks][ct] = *(LAS v4u*)(L + w * 16384 + 8192 + (ks * 4 + ct) * 1024 + lane * 16);
                nap(isRed ? D_E6R : (isGU ? D_E6G : D_E6));
                D2x8 dd;
                poll2(c, dd, [&](D2x8& d) { d.v = ld8(c, WS_E6 + tid * 8); d.s = ld8(c, WS_E6); }, [&](const D2x8& d) { return okbit<1u>(d.v.x & d.v.y & d.s.x & d.s.y); });
                sh0 = u2f(dd.s.x); sh1 = u2f(dd.s.y); x0 = u2f(dd.v.x) - sh0; x1 = u2f(dd.v.y) - sh1;
                STAMP(20);
            }
            ln_gemv_partials(x0, x1, wqk, L, GA, lane);
            __syncthreads();
            if constexpr (layer == 0) {
                if (w == 7 && cu == 0) {
                    v2u mv[4];
                    int spins = 0;
                    for (;;) {
#pragma unroll
                        for (int k = 0; k < 4; ++k) mv[k] = ld8(c, WS_HW + (lane + 64 * k) * 128);
                        if (__all((mv[0].x == mo[0] || lane == 0) && mv[1].x == mo[1] && mv[2].x == mo[2] && mv[3].x == mo[3])) break;
                        if (give_up(c, spins)) break;
                    }
#pragma unroll
                    for (int k = 0; k < 4; ++k) st4(c, WS_HR + lane * 4 + k * 256, mv[k].y);
                    ckv_rows();
                }
            }
            if (w < 4) {
                float o[4];
                ln_gemv_final(L, GA, sh0, sh1, o);
                const float SC = 0.08838834764831845f * 1.4426950408889634f;
                if ((lane & 7) == 0) {
                    st4(c, dst, pack2_tag<T>(o[0] * SC, o[2] * SC));
                    st4(c, dst + 32768, pack2_tag<T>(o[1] * SC, o[3] * SC));
                }
            }
            STAMP(2 + 20 * layer);
        }
        v4u wuvr[2];
        {
            IDS();
            if constexpr (layer == 0) {
                {
                    const u16* pu = p.wuv + (size_t)(64 * j1 + (tid >> 4)) * HD + h1 * 128 + (tid & 15) * 8;
                    const u16* pu2 = pu + (size_t)32 * HD;
                    LDNT_ASM(wuvr[0], pu); LDNT_ASM(wuvr[1], pu2);
                }
                asm volatile("s_waitcnt vmcnt(2)" ::: "memory");
                STAMP(19);
                __syncthreads();
                const int fs = swz16(n);
#pragma unroll
                for (int s = 0; s < 16; ++s) ck[s] = *(LAS v4u*)(L + (16 * w + n) * 1024 + (((4 * s + g) ^ fs) * 16));
                v2u tr[4][8];
                const int q = n >> 2, pp = n & 3;
#pragma unroll
                for (int t = 0; t < 2; ++t) {
                    const int r = 8 * g + 4 * t + q;
#pragma unroll
                    for (int ct = 0; ct < 4; ++ct) {
                        const unsigned ad = lds0 + r * 1024 + (((8 * w + 2 * ct + (pp >> 1)) ^ swz16(r & 15)) * 16) + 8 * (pp & 1);
                        TR_ISSUE(tr[ct][0 + t], ad, 0); TR_ISSUE(tr[ct][2 + t], ad, 32768);
                        const unsigned ad2 = ad + 65536;
                        TR_ISSUE(tr[ct][4 + t], ad2, 0); TR_ISSUE(tr[ct][6 + t], ad2, 32768);
                    }
                }
                asm volatile("s_waitcnt lgkmcnt(0)" : "+v"(tr[0][0]), "+v"(tr[0][1]), "+v"(tr[0][2]), "+v"(tr[0][3]), "+v"(tr[0][4]), "+v"(tr[0][5]), "+v"(tr[0][6]), "+v"(tr[0][7]),
                             "+v"(tr[1][0]), "+v"(tr[1][1]), "+v"(tr[1][2]), "+v"(tr[1][3]), "+v"(tr[1][4]), "+v"(tr[1][5]), "+v"(tr[1][6]), "+v"(tr[1][7]) :: "memory");
                asm volatile("" : "+v"(tr[2][0]), "+v"(tr[2][1]), "+v"(tr[2][2]), "+v"(tr[2][3]), "+v"(tr[2][4]), "+v"(tr[2][5]), "+v"(tr[2][6]), "+v"(tr[2][7]),
                             "+v"(tr[3][0]), "+v"(tr[3][1]), "+v"(tr[3][2]), "+v"(tr[3][3]), "+v"(tr[3][4]), "+v"(tr[3][5]), "+v"(tr[3][6]), "+v"(tr[3][7]) :: "memory");
#pragma unroll
                for (int ct = 0; ct < 4; ++ct)
#pragma unroll
                    for (int s = 0; s < 4; ++s) ckT[ct][s] = v4u{tr[ct][2 * s].x, tr[ct][2 * s].y, tr[ct][2 * s + 1].x, tr[ct][2 * s + 1].y};
#pragma unroll
                for (int s = 0; s < 16; ++s) pin(ck[s]);
                __syncthreads();
                STAMP(17);
                if (w == 6) {
                    int spins = 0;
                    while (cu != 0) {
                        const unsigned r = ld4(c, WS_HR + cu * 4), m = ld4(c, WS_HM + cu * 4);
                        if (r == hc) break;
                        if (m != h0) { h0 = m; if (lane == 0) st8(c, WS_HW + cu * 128, v2u{h0, hc}); }
                        if (give_up(c, spins)) break;
                    }
                    *(volatile LAS unsigned*)(L + L_GO) = 1u;
                }
                if (w >= 4) { wo_rows(w - 4); wo_rows(w); }
                else { while (*(volatile LAS unsigned*)(L + L_GO) == 0u) __builtin_amdgcn_s_sleep(1); }
            }
            if (w < 4) {
                const int t = tid;
                v4u qg[4];
                const int e1 = WS_E1 + (bb * 2 + hg) * 16384 + t * 16;
                nap(layer == 0 ? D_E1A : D_E1);
                D4x16 dd;
                poll2(c, dd, [&](D4x16& d) {
#pragma unroll
                    for (int k = 0; k < 4; ++k) d.d[k] = ld16(c, e1 + k * 4096); },
                    [&](const D4x16& d) { return okbit<T>(andor<T>(andor<T>(v4<T>(d.d[0]), v4<T>(d.d[1])), andor<T>(v4<T>(d.d[2]), v4<T>(d.d[3])))); });
#pragma unroll
                for (int k = 0; k < 4; ++k) qg[k] = dd.d[k];
#pragma unroll
                for (int k = 0; k < 4; ++k) { const int u = t + 256 * k; *(LAS v4u*)(L + L_A + (u >> 6) * 1040 + (u & 63) * 16) = qg[k]; }
            }
        }
        {
            IDS();
            int e2a = WS_E2 + ((2 * (n & 7) + hg) * 8 + w) * 16384 + (bb * 64 + split) * 128 + 16 * g + (n < 8 ? 0 : 64);
            int e2b = WS_E2 + ((2 * ((n & 7) + 8) + hg) * 8 + w) * 16384 + (bb * 64 + split) * 128 + 16 * g + (n < 8 ? 64 : 0);
            int qab = L_A + n * 1040 + 16 * g, smw = L_SMAX + (n * 8 + w) * 4, smr = L_SMAX + n * 32;
            int pw_ = L_P + n * 272 + (16 * w + 4 * g) * 2, pr_ = L_P + n * 272 + 16 * g;
            pini(qab); pini(e2a); pini(e2b); pini(smw); pini(smr); pini(pw_); pini(pr_);
            __syncthreads();
            STAMP(3 + 20 * layer);
            f32x4 s0 = Z4, s1 = Z4;
            {
                v4u q[8];
#pragma unroll
                for (int s = 0; s < 8; ++s) q[s] = *(LAS v4u*)(L + qab + 64 * s);
                FENCE();
#pragma unroll
                for (int s = 0; s < 8; s += 2) {
                    s0 = mfma(ck[s], q[s], s0); s1 = mfma(ck[s + 1], q[s + 1], s1);
                    q[s] = *(LAS v4u*)(L + qab + 64 * (s + 8)); q[s + 1] = *(LAS v4u*)(L + qab + 64 * (s + 9));
                }
#pragma unroll
                for (int s = 0; s < 8; s += 2) { s0 = mfma(ck[s + 8], q[s], s0); s1 = mfma(ck[s + 9], q[s + 1], s1); }
            }
            s0 += s1;
            float mx = fmaxf(fmaxf(s0.x, s0.y), fmaxf(s0.z, s0.w));
            mx = x32_max(x16_max(mx));
            if (g == 0) *(LAS float*)(L + smw) = mx;
            __syncthreads();
            if constexpr (layer == 0) {
                asm volatile("s_cmp_lt_u32 %2, 4\n\ts_cbranch_scc1 1f\n\ts_waitcnt vmcnt(%3)\n\ts_branch 2f\n1:\n\ts_waitcnt vmcnt(0)\n2:" : "+v"(wuvr[0]), "+v"(wuvr[1]) : "s"(w), "n"(32) : "memory", "scc");
                const int r = tid >> 4;
                LAS unsigned char* dst = L + L_A + r * 256 + (((tid & 15) ^ swz16(r & 15)) * 16);
                *(LAS v4u*)dst = wuvr[0]; *(LAS v4u*)(dst + 32 * 256) = wuvr[1];
            }
            const f32x4 ma = *(LAS f32x4*)(L + smr), mb = *(LAS f32x4*)(L + smr + 16);
            const float M = fmaxf(fmaxf(fmaxf(ma.x, ma.y), fmaxf(ma.z, ma.w)), fmaxf(fmaxf(mb.x, mb.y), fmaxf(mb.z, mb.w)));
            const unsigned p01 = pack2(__builtin_amdgcn_exp2f(s0.x - M), __builtin_amdgcn_exp2f(s0.y - M));
            const unsigned p23 = pack2(__builtin_amdgcn_exp2f(s0.z - M), __builtin_amdgcn_exp2f(s0.w - M));
            *(LAS v2u*)(L + pw_) = v2u{p01, p23};
            if constexpr (layer == 0) asm volatile("s_waitcnt vmcnt(%0)" :: "n"(16) : "memory");
            __syncthreads();
            v4u pf[4];
#pragma unroll
            for (int s = 0; s < 4; ++s) pf[s] = *(LAS v4u*)(L + pr_ + 64 * s);
            const int h = 2 * n + hg;
            v4u ov[2];
#pragma unroll
            for (int q = 0; q < 2; ++q) {
                f32x4 a = Z4, b = Z4;
#pragma unroll
                for (int s = 0; s < 4; ++s) { a = mfma(ckT[2 * q][s], pf[s], a); b = mfma(ckT[2 * q + 1][s], pf[s], b); }
                ov[q] = v4u{pack2_tag<T>(a.x, a.y), pack2_tag<T>(a.z, a.w), pack2_tag<T>(b.x, b.y), pack2_tag<T>(b.z, b.w)};
            }
            {
                const v4u r1 = {dppu<0x128>(ov[1].x), dppu<0x128>(ov[1].y), dppu<0x128>(ov[1].z), dppu<0x128>(ov[1].w)};
                const bool lo8 = n < 8;
                st16(c, e2a, lo8 ? ov[0] : r1);
                st16(c, e2b, lo8 ? r1 : ov[0]);
            }
            if (w == 0) {
                const v4u ones = {0x3f803f80u, 0x3f803f80u, 0x3f803f80u, 0x3f803f80u};
                f32x4 l = Z4;
#pragma unroll
                for (int s = 0; s < 4; ++s) l = mfma(ones, pf[s], l);
                if (g == 0) st8(c, WS_ST + ((bb * 32 + h) * 64 + split) * 8, v2u{tagf<T>(M), tagf<T>(l.x)});
            }
            STAMP(4 + 20 * layer);
        }
        v4u wvo[2][4];
        if constexpr (layer == 0) {
            IDS();
            if (w >= 4) asm volatile("s_waitcnt vmcnt(0)" ::: "memory");
            v2u tr[4][8];
            const int q = n >> 2, pp = n & 3;
#pragma unroll
            for (int t = 0; t < 2; ++t) {
                const int d = 8 * g + 4 * t + q;
#pragma unroll
                for (int nt = 0; nt < 4; ++nt) {
                    const unsigned ad = lds0 + w * 16384 + d * 128 + (((2 * nt + (pp >> 1)) ^ swz8(d)) * 16) + 8 * (pp & 1);
                    TR_ISSUE(tr[nt][0 + t], ad, 0); TR_ISSUE(tr[nt][2 + t], ad, 4096); TR_ISSUE(tr[nt][4 + t], ad, 8192); TR_ISSUE(tr[nt][6 + t], ad, 12288);
                }
            }
            asm volatile("s_waitcnt lgkmcnt(0)" : "+v"(tr[0][0]), "+v"(tr[0][1]), "+v"(tr[0][2]), "+v"(tr[0][3]), "+v"(tr[0][4]), "+v"(tr[0][5]), "+v"(tr[0][6]), "+v"(tr[0][7]),
                         "+v"(tr[1][0]), "+v"(tr[1][1]), "+v"(tr[1][2]), "+v"(tr[1][3]), "+v"(tr[1][4]), "+v"(tr[1][5]), "+v"(tr[1][6]), "+v"(tr[1][7]) :: "memory");
            asm volatile("" : "+v"(tr[2][0]), "+v"(tr[2][1]), "+v"(tr[2][2]), "+v"(tr[2][3]), "+v"(tr[2][4]), "+v"(tr[2][5]), "+v"(tr[2][6]), "+v"(tr[2][7]),
                         "+v"(tr[3][0]), "+v"(tr[3][1]), "+v"(tr[3][2]), "+v"(tr[3][3]), "+v"(tr[3][4]), "+v"(tr[3][5]), "+v"(tr[3][6]), "+v"(tr[3][7]) :: "memory");
#pragma unroll
            for (int ks = 0; ks < 2; ++ks)
#pragma unroll
                for (int ct = 0; ct < 4; ++ct) *(LAS v4u*)(L + w * 16384 + 8192 + (ks * 4 + ct) * 1024 + lane * 16) = wqk[ks][ct];
            const int fs = swz16(n);
#pragma unroll
            for (int ks = 0; ks < 2; ++ks) {
                v4u u0[4], u1[4];
#pragma unroll
                for (int s = 0; s < 4; ++s) {
                    u0[s] = *(LAS v4u*)(L + L_A + (32 * ks + n) * 256 + (((4 * s + g) ^ fs) * 16));
                    u1[s] = *(LAS v4u*)(L + L_A + (32 * ks + 16 + n) * 256 + (((4 * s + g) ^ fs) * 16));
                }
#pragma unroll
                for (int nt = 0; nt < 4; ++nt) {
                    f32x4 a0 = Z4, a1 = Z4;
#pragma unroll
                    for (int s = 0; s < 4; ++s) {
                        const v4u bo = v4u{tr[nt][2 * s].x, tr[nt][2 * s].y, tr[nt][2 * s + 1].x, tr[nt][2 * s + 1].y};
                        a0 = mfma(u0[s], bo, a0); a1 = mfma(u1[s], bo, a1);
                    }
                    *(LAS v4u*)(L + w * 16384 + (ks * 4 + nt) * 1024 + lane * 16) =
                        v4u{pack2(a0.x, a0.y), pack2(a0.z, a0.w), pack2(a1.x, a1.y), pack2(a1.z, a1.w)};
                }
                FENCE();
            }
            if (w < 4) {
                    const u16* pb = tid < 56 ? p.w2 + (size_t)(56 * idx + tid) * MD + 64 * xcd
                                             : ((tid < 136 ? p.w1 : p.w3) + (size_t)(16 * idx + ((tid - 56) % 80) / 5) * FF + min(224 * xcd + 64 * (((tid - 56) % 80) % 5), FF - 2));
                    asm volatile("global_load_dword %0, %1, off" : "=v"(warm) : "v"(pb) : "memory");
            }
            __syncthreads();
        }
        STAMP(5 + 20 * layer);
        {
            IDS();
#pragma unroll
            for (int ks = 0; ks < 2; ++ks)
#pragma unroll
                for (int nt = 0; nt < 4; ++nt) wvo[ks][nt] = *(LAS v4u*)(L + w * 16384 + (ks * 4 + nt) * 1024 + lane * 16);
            if constexpr (layer == 1) nap(D_E2);
            const int b = w >> 2, chunk = lane & 7, sp0 = (tid >> 3) & 31;
            const int ib = WS_E2 + (h1 * 8 + j1) * 16384 + (b * 64 + sp0) * 128 + chunk * 16;
            const int sb = WS_ST + ((b * 32 + h1) * 64 + lane) * 8;
            const FragAddr FA = frag_addr<false>(w, lane);
            int imo = L_A + (b * 64 + sp0) * 128 + ((chunk ^ swz8(sp0)) * 16);
            int tra = (int)lds0 + L_A + (b * 64 + 8 * g + (n >> 2)) * 128 + (((2 * (w & 3) + ((n & 3) >> 1)) ^ swz8(8 * g + (n >> 2))) * 16) + 8 * (n & 1);
            int x2w = L_X2 + (b * 32 + 8 * (w & 3) + (n >> 1)) * 4;
            int ofr = (n < 4 ? L_X2 + n * 128 : L_CST + 128) + 16 * g;
            int e3d = WS_E3 + 4 * w * 32768 + cu * 128 + n * 8;
            pini(imo); pini(tra); pini(x2w); pini(ofr); pini(e3d);
            v4u d0, d1; v2u ls;
            int spins = 0;
            for (;;) {
                d0 = ld16(c, ib); d1 = ld16(c, ib + 32 * 128); ls = ld8(c, sb);
                if (__all(okbit<T>(andor<T>(andor<T>(v4<T>(d0), v4<T>(d1)), andor<T>(ls.x, ls.y))))) break;
                if (give_up(c, spins)) break;
            }
            STAMP(6 + 20 * layer);
            if constexpr (layer == 0) {
                asm volatile("" ::"v"(warm));
            }
            *(LAS v4u*)(L + imo) = d0; *(LAS v4u*)(L + imo + 32 * 128) = d1;
            const float Mi = u2f(ls.x);
            const float e = __builtin_amdgcn_exp2f(Mi - wave_max(Mi));
            const float wn = e * __builtin_amdgcn_rcpf(wave_sum(e * u2f(ls.y)));
            v4u wf[2];
            mk_frags(wn, 0.f, L, FA, lane & 1, wf);
            __syncthreads();
            {
                v2u t0, t1, t2, t3;
                asm volatile("ds_read_b64_tr_b16 %0, %4\n\tds_read_b64_tr_b16 %1, %4 offset:512\n\tds_read_b64_tr_b16 %2, %4 offset:4096\n\tds_read_b64_tr_b16 %3, %4 offset:4608\n\ts_waitcnt lgkmcnt(0)"
                             : "=&v"(t0), "=&v"(t1), "=&v"(t2), "=&v"(t3) : "v"(tra) : "memory");
                f32x4 o = mfma(wf[0], v4u{t0.x, t0.y, t1.x, t1.y}, Z4);
                o = mfma(wf[1], v4u{t2.x, t2.y, t3.x, t3.y}, o);
                const float v = o.x + o.z;
                const float vn = dpp<0xB1>(v);
                const unsigned hi = pack2(v, vn);
                const unsigned lo = pack2(v - bflo(hi), vn - bfhi(hi));
                LAS unsigned* x2 = (LAS unsigned*)(L + x2w);
                if ((lane & 0x31) == 0) { x2[0] = hi; x2[64] = lo; }
            }
            __syncthreads();
            const v4u of0 = *(LAS v4u*)(L + ofr), of1 = *(LAS v4u*)(L + ofr + 64);
            f32x4 a2[4];
#pragma unroll
            for (int nt = 0; nt < 4; ++nt) a2[nt] = mfma(of0, wvo[0][nt], Z4);
#pragma unroll
            for (int nt = 0; nt < 4; ++nt) a2[nt] = mfma(of1, wvo[1][nt], a2[nt]);
            if (g == 0) {
#pragma unroll
                for (int nt = 0; nt < 4; ++nt) st8(c, e3d + nt * 32768, v2u{tagf<T>(a2[nt].x + a2[nt].z), tagf<T>(a2[nt].y + a2[nt].w)});
            }
            STAMP(7 + 20 * layer);
        }
        auto ident = [&](int n, int g, int hi) {
            unsigned a[4];
#pragma unroll
            for (int q = 0; q < 4; ++q) { const int k0 = 8 * g + 2 * q - 16 * hi; a[q] = (k0 == n ? 0x3f80u : 0u) | (k0 + 1 == n ? 0x3f800000u : 0u); }
            return v4u{a[0], a[1], a[2], a[3]};
        };
        if (isRed) {
            IDS();
            nap(D_E3);
            const int ib = WS_E3 + red * 32768 + tid * 16;
            int rdw = L_RED + w * 128 + lane * 16, rdr = L_RED + (lane & 7) * 128 + (lane >> 3) * 16, e4d = WS_E4 + red * 128 + (lane >> 3) * 16;
            pini(rdw); pini(rdr); pini(e4d);
            v4u d[4];
            {
                D4x16 dd;
                poll2(c, dd, [&](D4x16& q) {
#pragma unroll
                    for (int k = 0; k < 4; ++k) q.d[k] = ld16(c, ib + k * 8192); },
                    [&](const D4x16& q) { return okbit<T>(andor<T>(andor<T>(v4<T>(q.d[0]), v4<T>(q.d[1])), andor<T>(v4<T>(q.d[2]), v4<T>(q.d[3])))); });
#pragma unroll
                for (int k = 0; k < 4; ++k) d[k] = dd.d[k];
            }
            STAMP(8 + 20 * layer);
            float a[4];
            a[0] = (u2f(d[0].x) + u2f(d[1].x)) + (u2f(d[2].x) + u2f(d[3].x));
            a[1] = (u2f(d[0].y) + u2f(d[1].y)) + (u2f(d[2].y) + u2f(d[3].y));
            a[2] = (u2f(d[0].z) + u2f(d[1].z)) + (u2f(d[2].z) + u2f(d[3].z));
            a[3] = (u2f(d[0].w) + u2f(d[1].w)) + (u2f(d[2].w) + u2f(d[3].w));
#pragma unroll
            for (int i = 0; i < 4; ++i) { a[i] += dpp<0x128>(a[i]); a[i] = x32_sum(x16_sum(a[i])); }
            if (lane < 8) *(LAS f32x4*)(L + rdw) = f32x4{a[0], a[1], a[2], a[3]};
            __syncthreads();
            if (w == 0) {
                const f32x4 v = *(LAS f32x4*)(L + rdr);
                xres += f32x4{sum8(v.x), sum8(v.y), sum8(v.z), sum8(v.w)};
                if ((lane & 7) == 0) st16(c, e4d, v4u{tagf<T>(xres.x), tagf<T>(xres.y), tagf<T>(xres.z), tagf<T>(xres.w)});
            }
            STAMP(9 + 20 * layer);
            if constexpr (layer == 0) {
                const u16* pw = p.w2 + (size_t)(224 * w + 16 * (g >> 1) + n) * MD + 16 * red + 8 * (g & 1);
                const v4u Ilo = ident(n, g, 0), Ihi = ident(n, g, 1);
                v4u raw[7];
#pragma unroll
                for (int ks = 0; ks < 7; ++ks) raw[ks] = ldnt(pw + (size_t)ks * 32 * MD);
                FENCE();
#pragma unroll
                for (int ks = 0; ks < 7; ++ks) {
                    const f32x4 a0 = mfma(raw[ks], Ilo, Z4), a1 = mfma(raw[ks], Ihi, Z4);
                    glf[ks >> 2][ks & 3] = v4u{pack2(a0.x, a0.y), pack2(a0.z, a0.w), pack2(a1.x, a1.y), pack2(a1.z, a1.w)};
                    pin(glf[ks >> 2][ks & 3]);
                }
            }
        }
        if (isGU) {
            IDS();
            if constexpr (layer == 0) {
                const v4u Ilo = ident(n, g, 0), Ihi = ident(n, g, 1);
                v4u raw[2][4];
#pragma unroll
                for (int mt = 0; mt < 4; ++mt) {
                    raw[0][mt] = ldnt(p.w1 + (size_t)(64 * w + 16 * mt + n) * FF + 32 * gu + 8 * g);
                    raw[1][mt] = ldnt(p.w3 + (size_t)(64 * w + 16 * mt + n) * FF + 32 * gu + 8 * g);
                }
                FENCE();
#pragma unroll
                for (int mat = 0; mat < 2; ++mat)
#pragma unroll
                    for (int ks = 0; ks < 2; ++ks) {
                        const f32x4 l0 = mfma(raw[mat][2 * ks], Ilo, Z4), h0 = mfma(raw[mat][2 * ks], Ihi, Z4);
                        const f32x4 l1 = mfma(raw[mat][2 * ks + 1], Ilo, Z4), hh1 = mfma(raw[mat][2 * ks + 1], Ihi, Z4);
                        glf[ks][mat * 2] = v4u{pack2(l0.x, l0.y), pack2(l0.z, l0.w), pack2(l1.x, l1.y), pack2(l1.z, l1.w)};
                        glf[ks][mat * 2 + 1] = v4u{pack2(h0.x, h0.y), pack2(h0.z, h0.w), pack2(hh1.x, hh1.y), pack2(hh1.z, hh1.w)};
                        pin(glf[ks][mat * 2]); pin(glf[ks][mat * 2 + 1]);
                    }
            } else nap(D_E4);
            const int pair = 4 * (w & 3) + ((lane >> 3) & 3) + 16 * (lane >> 5);
            const GemvAddr GA = gemv_addr(w, lane, pair);
            int e5d = WS_E5 + gu * 256 + (pair & 15) * 16;
            pini(e5d);
            float x0, x1, sh0, sh1;
            {
                D2x8 dd;
                poll2(c, dd, [&](D2x8& d) { d.v = ld8(c, WS_E4 + tid * 8); d.s = ld8(c, WS_E4); },
                      [&](const D2x8& d) { return okbit<T>(andor<T>(andor<T>(d.v.x, d.v.y), andor<T>(d.s.x, d.s.y))); });
                sh0 = u2f(dd.s.x); sh1 = u2f(dd.s.y); x0 = u2f(dd.v.x) - sh0; x1 = u2f(dd.v.y) - sh1;
            }
            STAMP(10 + 20 * layer);
            ln_gemv_partials(x0, x1, glf, L, GA, lane);
            __syncthreads();
            if (w < 4) {
                float o[4];
                ln_gemv_final(L, GA, sh0, sh1, o);
                float hv[4];
#pragma unroll
                for (int i = 0; i < 4; ++i) {
                    auto r = __builtin_amdgcn_permlane32_swap(f2u(o[i]), f2u(o[i]), false, false);
                    const float gt = u2f(r[0]), up = u2f(r[1]);
                    hv[i] = gt * __builtin_amdgcn_rcpf(1.0f + __builtin_amdgcn_exp2f(-1.4426950408889634f * gt)) * up;
                }
                if ((lane & 0x27) == 0) st16(c, e5d, v4u{tagf<T>(hv[0]), tagf<T>(hv[1]), tagf<T>(hv[2]), tagf<T>(hv[3])});
            }
            STAMP(11 + 20 * layer);
        }
        if (isRed) {
            IDS();
            if constexpr (layer == 1) nap(D_E5);
            const int ib = WS_E5 + (112 * w + lane) * 16;
            const int ib1 = ib + (lane < 48 ? 1024 : 0);
            int hw, hr = (n < 4 ? L_A + w * 1856 + n * 464 : L_CST + 128) + 16 * g;
            { const int f = 2 * lane; hw = L_A + w * 1856 + ((f & ~31) + 8 * ((f >> 2) & 3) + 4 * ((f >> 4) & 1) + (f & 3)) * 2; }
            int rdw = L_RED + w * 128 + n * 8, rdr = L_RED + (lane & 7) * 128 + (lane >> 3) * 16, e6d = WS_E6 + red * 128 + (lane >> 3) * 16;
            pini(hw); pini(hr); pini(rdw); pini(rdr); pini(e6d);
            v4u d0, d1;
            {
                D2x16 dd;
                poll2(c, dd, [&](D2x16& d) { d.d0 = ld16(c, ib); d.d1 = ld16(c, ib1); }, [&](const D2x16& d) { return okbit<T>(andor<T>(v4<T>(d.d0), v4<T>(d.d1))); });
                d0 = dd.d0; d1 = dd.d1;
            }
            STAMP(12 + 20 * layer);
#pragma unroll
            for (int k = 0; k < 2; ++k) {
                const v4u d = k ? d1 : d0;
                const unsigned h0 = pack2(u2f(d.x), u2f(d.z)), hb1 = pack2(u2f(d.y), u2f(d.w));
                const unsigned l0 = pack2(u2f(d.x) - bflo(h0), u2f(d.z) - bfhi(h0)), lb1 = pack2(u2f(d.y) - bflo(hb1), u2f(d.w) - bfhi(hb1));
                LAS unsigned char* hs = L + hw + 256 * k;
                if (k == 0 || lane < 48) {
                    *(LAS unsigned*)hs = h0; *(LAS unsigned*)(hs + 464) = hb1;
                    *(LAS unsigned*)(hs + 928) = l0; *(LAS unsigned*)(hs + 1392) = lb1;
                }
            }
            f32x4 a = Z4, a2 = Z4;
#pragma unroll
            for (int ks = 0; ks < 7; ++ks) {
                const v4u v = *(LAS v4u*)(L + hr + 64 * ks);
                if (ks & 1) a2 = mfma(v, glf[ks >> 2][ks & 3], a2); else a = mfma(v, glf[ks >> 2][ks & 3], a);
            }
            a += a2;
            if (g == 0) *(LAS f32x2*)(L + rdw) = f32x2{a.x + a.z, a.y + a.w};
            __syncthreads();
            if (w == 0) {
                const f32x4 v = *(LAS f32x4*)(L + rdr);
                xres += f32x4{sum8(v.x), sum8(v.y), sum8(v.z), sum8(v.w)};
                if ((lane & 7) == 0) {
                    if constexpr (layer == 0) st16(c, e6d, v4u{tagf<1u>(xres.x), tagf<1u>(xres.y), tagf<1u>(xres.z), tagf<1u>(xres.w)});
                    else {
                        GAS unsigned* po = (GAS unsigned*)(p.out + 16 * red + 2 * (lane >> 3));
                        po[0] = pack2(xres.x, xres.z); po[256] = pack2(xres.y, xres.w);
                    }
                }
            }
            STAMP(13 + 20 * layer);
        }
    };
    layer_body(std::integral_constant<int, 0>{});
    layer_body(std::integral_constant<int, 1>{});
    asm volatile("mk_code_end:");
}
}

extern "C" void kernel_launch(void* const* d_in, const int* in_sizes, int n_in,
                              void* d_out, int out_size, void* d_ws, size_t ws_size,
                              hipStream_t stream) {
    mk::Params p;
    p.x = (const u16*)d_in[0]; p.ckv = (const u16*)d_in[1]; p.wq = (const u16*)d_in[2]; p.wuk = (const u16*)d_in[3];
    p.wuv = (const u16*)d_in[4]; p.wo = (const u16*)d_in[5]; p.w1 = (const u16*)d_in[7]; p.w3 = (const u16*)d_in[8];
    p.w2 = (const u16*)d_in[9]; p.out = (u16*)d_out; p.ws = (unsigned char*)d_ws;
    hipLaunchKernelGGL(mk::mega, dim3(mk::NWG), dim3(mk::NT), 0, stream, p);
}
```
